```python
import torch

def _softmax(x, axis=-1):
    return torch.softmax(x, dim=axis)
def _gelu(x):
    return torch.nn.functional.gelu(x, approximate="tanh")
def _silu(x):
    return torch.nn.functional.silu(x)
def _sigmoid(x):
    return torch.sigmoid(x)
def _layernorm(x, axis=-1, eps=1e-5):
    m = x.mean(dim=axis, keepdim=True)
    v = x.var(dim=axis, keepdim=True, unbiased=False)
    return (x - m) / torch.sqrt(v + eps)
def _scatter_add(tgt, idx, src, axis=0):
    return tgt.clone().index_add_(axis, idx, src)
def _segment_sum(src, seg, n):
    out = torch.zeros((n,) + src.shape[1:], dtype=src.dtype)
    return out.index_add_(0, seg, src)
def _pad(x, pw):
    flat = tuple(v for pair in reversed(pw) for v in pair)
    return torch.nn.functional.pad(x, flat)

kernel_name = 'gated_mix__sliding_local_dilated__sliding_local_dilated__prompt'

def setup_inputs():

    _w = (lambda a: (lambda t: t.to(torch.bfloat16) if t.is_floating_point() else t)(torch.as_tensor(a)))
    def _A_setup():
        B, S, D = 1, 2048, 640
        return {
            "x": _w((lambda *s: torch.randn(*s, dtype=torch.bfloat16) * 0.05)(B, S, D)),
            "wqkv": _w((lambda *s: torch.randn(*s, dtype=torch.bfloat16) * 0.8)(D, 3 * D)),
            "wo": _w((lambda *s: torch.randn(*s, dtype=torch.bfloat16) * 0.05)(D, D)),
        }
    _A_in = _A_setup()

    def _B_setup():
        B, S, D = 1, 2048, 640
        return {
            "x": _w((lambda *s: torch.randn(*s, dtype=torch.bfloat16) * 0.05)(B, S, D)),
            "wqkv": _w((lambda *s: torch.randn(*s, dtype=torch.bfloat16) * 0.8)(D, 3 * D)),
            "wo": _w((lambda *s: torch.randn(*s, dtype=torch.bfloat16) * 0.05)(D, D)),
        }
    _B_in = _B_setup()

    return {
        "A_x": _A_in["x"],
        "A_wqkv": _A_in["wqkv"],
        "A_wo": _A_in["wo"],
        "B_x": _B_in["x"],
        "B_wqkv": _B_in["wqkv"],
        "B_wo": _B_in["wo"],
        "gate_w": _w((lambda *s: torch.randn(*s, dtype=torch.bfloat16) * 0.05)(640) * 16.0),
    }

def reference(A_x, A_wqkv, A_wo, B_x, B_wqkv, B_wo, gate_w):
    def _A_ref(x, wqkv, wo):
        B, S, D = x.shape
        H = 32; Dh = D // H
        W, dil = 8, 2
        qkv = (lambda x, *d: x.permute(*d))((x @ wqkv).reshape(B, S, 3, H, Dh), 2, 0, 3, 1, 4)
        q, k, v = qkv[0], qkv[1], qkv[2]
        scale = Dh ** -0.5
        pos = torch.arange(S)
        off = torch.arange(W) * dil
        idx = pos[:, None] - off[None, :]
        mask = idx >= 0
        idx_c = torch.where(mask, idx, 0).reshape(-1)
        kg = k[:, :, idx_c, :].reshape(B, H, S, W, Dh)
        vg = v[:, :, idx_c, :].reshape(B, H, S, W, Dh)
        logits = torch.einsum("bhsd,bhswd->bhsw", q, kg) * scale
        logits = torch.where(mask[None, None, :, :], logits, -1e9)
        attn = _softmax(logits, axis=-1)
        o = torch.einsum("bhsw,bhswd->bhsd", attn, vg)
        return (lambda x, *d: x.permute(*d))(o, 0, 2, 1, 3).reshape(B, S, D) @ wo

    def _B_ref(x, wqkv, wo):
        B, S, D = x.shape
        H = 32; Dh = D // H
        W, dil = 8, 2
        qkv = (lambda x, *d: x.permute(*d))((x @ wqkv).reshape(B, S, 3, H, Dh), 2, 0, 3, 1, 4)
        q, k, v = qkv[0], qkv[1], qkv[2]
        scale = Dh ** -0.5
        pos = torch.arange(S)
        off = torch.arange(W) * dil
        idx = pos[:, None] - off[None, :]
        mask = idx >= 0
        idx_c = torch.where(mask, idx, 0).reshape(-1)
        kg = k[:, :, idx_c, :].reshape(B, H, S, W, Dh)
        vg = v[:, :, idx_c, :].reshape(B, H, S, W, Dh)
        logits = torch.einsum("bhsd,bhswd->bhsw", q, kg) * scale
        logits = torch.where(mask[None, None, :, :], logits, -1e9)
        attn = _softmax(logits, axis=-1)
        o = torch.einsum("bhsw,bhswd->bhsd", attn, vg)
        return (lambda x, *d: x.permute(*d))(o, 0, 2, 1, 3).reshape(B, S, D) @ wo

    x = A_x
    g = _sigmoid((x * gate_w).sum(-1, keepdims=True))
    a = _A_ref(x, A_wqkv, A_wo)
    b = _B_ref(x, B_wqkv, B_wo)
    return g * a + (1 - g) * b
```

```cpp
#include <hip/hip_runtime.h>
#include <hip/hip_bf16.h>
typedef __hip_bfloat16 bf16;
constexpr int S=2048, D=640, H=32, DH=20;
__device__ inline float b2f(bf16 v){return __bfloat162float(v);}
__global__ void k_qkv(const bf16* x,const bf16* wa,const bf16* wb,bf16* qkv){
  int n=blockIdx.x*256+threadIdx.x; int s=blockIdx.y; int br=blockIdx.z;
  if(n>=1920) return;
  const bf16* w=br?wb:wa; float acc=0;
  for(int k=0;k<D;k++) acc+=b2f(x[s*D+k])*b2f(w[k*1920+n]);
  qkv[((size_t)br*S+s)*1920+n]=__float2bfloat16(acc);
}
__global__ void k_attn(const bf16* qkv,bf16* o){
  int h=threadIdx.x&31; int s=blockIdx.x*8+(threadIdx.x>>5); int br=blockIdx.y;
  const bf16* base=qkv+(size_t)br*S*1920;
  float l[8]; float m=-1e30f;
  for(int w=0;w<8;w++){int j=s-2*w; float a=0; if(j>=0){for(int d=0;d<DH;d++) a+=b2f(base[s*1920+h*DH+d])*b2f(base[j*1920+640+h*DH+d]); a*=0.22360679775f;} else a=-1e9f; l[w]=a; m=fmaxf(m,a);}
  float sum=0; for(int w=0;w<8;w++){l[w]=__expf(l[w]-m); sum+=l[w];}
  for(int d=0;d<DH;d++){float a=0; for(int w=0;w<8;w++){int j=s-2*w; if(j>=0) a+=l[w]*b2f(base[j*1920+1280+h*DH+d]);} o[((size_t)s*2+br)*D+h*DH+d]=__float2bfloat16(a/sum);}
}
__global__ void k_out(const bf16* x,const bf16* gw,const bf16* o,const bf16* woa,const bf16* wob,bf16* out){
  int n=blockIdx.x*128+threadIdx.x; int s=blockIdx.y;
  float g=0; for(int k=0;k<D;k++) g+=b2f(x[s*D+k])*b2f(gw[k]); g=1.f/(1.f+__expf(-g));
  float a=0,b=0;
  for(int k=0;k<D;k++){a+=b2f(o[(s*2)*D+k])*b2f(woa[k*D+n]); b+=b2f(o[(s*2+1)*D+k])*b2f(wob[k*D+n]);}
  out[s*D+n]=__float2bfloat16(g*a+(1.f-g)*b);
}
extern "C" void kernel_launch(void* const* d_in, const int* in_sizes, int n_in, void* d_out, int out_size, void* d_ws, size_t ws_size, hipStream_t stream){
  const bf16* x=(const bf16*)d_in[0]; const bf16* wa=(const bf16*)d_in[1]; const bf16* woa=(const bf16*)d_in[2];
  const bf16* wb=(const bf16*)d_in[4]; const bf16* wob=(const bf16*)d_in[5]; const bf16* gw=(const bf16*)d_in[6];
  bf16* qkv=(bf16*)d_ws; bf16* o=qkv+(size_t)2*S*1920;
  k_qkv<<<dim3(8,S,2),256,0,stream>>>(x,wa,wb,qkv);
  k_attn<<<dim3(S/8,2),256,0,stream>>>(qkv,o);
  k_out<<<dim3(5,S),128,0,stream>>>(x,gw,o,woa,wob,(bf16*)d_out);
}
```

```cpp
#include <hip/hip_runtime.h>
#include <stdint.h>

typedef __bf16 bf16x8 __attribute__((ext_vector_type(8)));
typedef __bf16 bf16x2 __attribute__((ext_vector_type(2)));
typedef short s16x4 __attribute__((ext_vector_type(4)));
typedef float f32x4 __attribute__((ext_vector_type(4)));
typedef float f32x2 __attribute__((ext_vector_type(2)));
typedef unsigned u32x2 __attribute__((ext_vector_type(2)));
typedef unsigned u32x4 __attribute__((ext_vector_type(4)));
#define LAS __attribute__((address_space(3)))
#define DI __device__ __forceinline__

constexpr int SLOT = 49152;
constexpr int XB = 18432;
constexpr int WKS = 15360;
constexpr int LDS_GATE = 3 * SLOT;
constexpr int LDS_ZERO = LDS_GATE + 2048;
constexpr int LDS1 = LDS_ZERO + 64;
constexpr int EXCH = SLOT;
constexpr int LDS2 = 163840;

constexpr size_t OMAIN_B = 4u << 20, OREM_B = 1u << 20;

DI bf16x8 lds_b128(unsigned a) { return __builtin_bit_cast(bf16x8, *(const LAS u32x4*)a); }
DI u32x2 lds_b64(unsigned a) { return *(const LAS u32x2*)a; }
DI u32x2 lds_tr(unsigned a) { return __builtin_bit_cast(u32x2, __builtin_amdgcn_ds_read_tr16_b64_v4i16((LAS s16x4*)a)); }
DI bf16x8 cat(u32x2 a, u32x2 b) { u32x4 r = {a.x, a.y, b.x, b.y}; return __builtin_bit_cast(bf16x8, r); }
DI bf16x8 lds_trfrag(unsigned a, int ustride) { return cat(lds_tr(a), lds_tr(a + ustride)); }
DI f32x4 mfma(bf16x8 a, bf16x8 b, f32x4 c) { return __builtin_amdgcn_mfma_f32_16x16x32_bf16(a, b, c, 0, 0, 0); }
DI unsigned pk(float a, float b) { f32x2 v = {a, b}; return __builtin_bit_cast(unsigned, __builtin_convertvector(v, bf16x2)); }
DI u32x2 pk4(f32x4 v) { u32x2 r = {pk(v[0], v[1]), pk(v[2], v[3])}; return r; }
DI void dma16(unsigned lds, unsigned voff, const void* base) {
  asm volatile("s_mov_b32 m0, %0\n\ts_nop 0\n\tglobal_load_lds_dwordx4 %1, %2" ::"s"(lds), "v"(voff), "s"(base) : "memory");
}
#define WAIT_BAR(n) asm volatile("s_waitcnt vmcnt(" #n ") lgkmcnt(0)\n\ts_barrier" ::: "memory")
DI void swap32(unsigned& a, unsigned& b) { auto r = __builtin_amdgcn_permlane32_swap(a, b, false, false); a = r[0]; b = r[1]; }
DI void swap16(unsigned& a, unsigned& b) { auto r = __builtin_amdgcn_permlane16_swap(a, b, false, false); a = r[0]; b = r[1]; }
DI u32x4 swap32x2(u32x2 a, u32x2 b) { unsigned ax = a.x, ay = a.y, bx = b.x, by = b.y; swap32(ax, bx); swap32(ay, by); u32x4 r = {ax, ay, bx, by}; return r; }
DI u32x4 swap16x2(u32x2 a, u32x2 b) { unsigned ax = a.x, ay = a.y, bx = b.x, by = b.y; swap16(ax, bx); swap16(ay, by); u32x4 r = {ax, ay, bx, by}; return r; }
DI void fswap32(float& a, float& b) { unsigned x = __float_as_uint(a), y = __float_as_uint(b); swap32(x, y); a = __uint_as_float(x); b = __uint_as_float(y); }
DI void fswap16(float& a, float& b) { unsigned x = __float_as_uint(a), y = __float_as_uint(b); swap16(x, y); a = __uint_as_float(x); b = __uint_as_float(y); }

template <int ROLE>
struct Frags {
  bf16x8 x[4], w[3], e[ROLE == 2 ? 4 : (ROLE == 1 ? 3 : 1)];
};

template <int ROLE>
DI void load_frags(Frags<ROLE>& f, unsigned xa, unsigned wm, unsigned wr, unsigned wa, unsigned xg, unsigned ga) {
#pragma unroll
  for (int t = 0; t < 4; ++t) f.x[t] = lds_b128(xa + t * 2048);
#pragma unroll
  for (int s = 0; s < 3; ++s) f.w[s] = lds_trfrag(wm + s * 160, 3840);
  f.e[0] = lds_trfrag(wr, 3840);
  if constexpr (ROLE == 1) { f.e[1] = lds_b128(xg); f.e[2] = lds_b128(ga); }
  if constexpr (ROLE == 2) { f.e[1] = lds_b128(xg); f.e[2] = lds_trfrag(wa + 160, 3840); f.e[3] = lds_trfrag(wa + 320, 3840); }
}
template <int ROLE>
DI void mma_step(const Frags<ROLE>& f, f32x4 (&aq)[4], f32x4 (&ak)[4], f32x4 (&av)[4], f32x4 (&ae)[5]) {
#pragma unroll
  for (int t = 0; t < 4; ++t) {
    aq[t] = mfma(f.w[0], f.x[t], aq[t]);
    ak[t] = mfma(f.w[1], f.x[t], ak[t]);
    av[t] = mfma(f.x[t], f.w[2], av[t]);
    if constexpr (ROLE != 2) ae[t] = mfma(f.e[0], f.x[t], ae[t]);
  }
  if constexpr (ROLE == 1) ae[4] = mfma(f.e[2], f.e[1], ae[4]);
  if constexpr (ROLE == 2) {
    ae[0] = mfma(f.w[1], f.e[1], ae[0]);
    ae[1] = mfma(f.w[2], f.e[1], ae[1]);
    ae[2] = mfma(f.e[2], f.e[1], ae[2]);
    ae[3] = mfma(f.e[3], f.e[1], ae[3]);
    ae[4] = mfma(f.e[0], f.e[1], ae[4]);
  }
}

template <int ROLE>
DI void kloop(f32x4 (&aq)[4], f32x4 (&ak)[4], f32x4 (&av)[4], f32x4 (&ae)[5], unsigned xa, unsigned wm, unsigned wr, unsigned wa,
              unsigned xg, unsigned ga, const unsigned (&voff)[6], const char* gbase, unsigned gstep, unsigned ldsw) {
  Frags<ROLE> f0, f1;
  WAIT_BAR(12);
  load_frags<ROLE>(f0, xa, wm, wr, wa, xg, ga);
  unsigned so = 0;
  for (int s = 0; s < 10; ++s) {
    load_frags<ROLE>(f1, (xa ^ 64) + so, wm + so + WKS, wr + so + WKS, wa + so + WKS, (xg ^ 64) + so, ga + 64);
    mma_step<ROLE>(f0, aq, ak, av, ae);
    __builtin_amdgcn_sched_barrier(0);
    if (s < 9) {
      if (s < 8) WAIT_BAR(6); else WAIT_BAR(0);
      if (s < 7) {
#pragma unroll
        for (int j = 0; j < 6; ++j) dma16(ldsw + so + j * 1024, voff[j], gbase);
        gbase += gstep;
      }
      so = (so == 2 * SLOT) ? 0 : so + SLOT;
      ga += 128;
      load_frags<ROLE>(f0, xa + so, wm + so, wr + so, wa + so, xg + so, ga);
    }
    mma_step<ROLE>(f1, aq, ak, av, ae);
    __builtin_amdgcn_sched_barrier(0);
  }
}

__global__ __launch_bounds__(512, 2) void k_qkv_attn(const char* __restrict__ x, const char* __restrict__ wA, const char* __restrict__ wB,
                                                     const char* __restrict__ gatew, char* __restrict__ ws) {
  const int tid = threadIdx.x, lane = tid & 63;
  const int w = __builtin_amdgcn_readfirstlane(tid >> 6);
  const int b = blockIdx.x, xcd = b & 7, idx = b >> 3;
  const int rt = (xcd >> 2) * 8 + (idx & 7), hg = (xcd & 3) * 4 + (idx >> 3);
  const int br = hg >> 3, hl = hg & 7;
  const int g = lane >> 4, i = lane & 15, q = i >> 2, pp = i & 3;
  const int p = w >> 2, h = w & 3;

  unsigned voff[6];
  const char* gbase;
  unsigned gstep;
  if (w < 3) {
    gbase = x; gstep = 128;
#pragma unroll
    for (int j = 0; j < 6; ++j) {
      int rho = (w * 6 + j) * 8 + (lane >> 3);
      int T = rho >> 4, ii = rho & 15;
      int row = (T < 8) ? rt * 128 + (T & 3) * 32 + 2 * ii + (T >> 2) : rt * 128 - 16 + 2 * (ii & 7) + (ii < 8 ? 1 : 0);
      row = row < 0 ? 0 : row;
      voff[j] = row * 1280 + (((lane & 7) ^ (ii >> 1)) << 4);
    }
  } else {
    gbase = (br ? wB : wA) + hl * 160; gstep = 64 * 3840;
#pragma unroll
    for (int j = 0; j < 6; ++j) {
      int u = ((w - 3) * 6 + j) * 64 + lane;
      int rho = u / 30, c = u - rho * 30;
      int kap = (rho & ~12) | ((rho & 4) << 1) | ((rho & 8) >> 1);
      int sct = c / 10;
      voff[j] = kap * 3840 + sct * 1280 + (c - sct * 10) * 16;
    }
  }
  const unsigned ldsw = w * 6144;
  if (w == 0) {
    dma16(LDS_GATE, lane * 16, gatew);
    dma16(LDS_GATE + 1024, 1024 + (lane & 15) * 16, gatew);
  }
#pragma unroll
  for (int s = 0; s < 3; ++s) {
#pragma unroll
    for (int j = 0; j < 6; ++j) dma16(ldsw + s * SLOT + j * 1024, voff[j], gbase);
    gbase += gstep;
  }
  if (tid < 16) *(LAS unsigned*)(LDS_ZERO + tid * 4) = 0u;

  const int sec = (0x12201102 >> (4 * w)) & 3;
  const unsigned xl = i * 128 + ((g ^ (i >> 1)) << 4);
  const unsigned xa = p * 8192 + xl;
  const unsigned wrow = XB + (16 * (g >> 1) + 4 * (g & 1) + q) * 480;
  const unsigned wm = wrow + 40 * h + 8 * pp;
  const unsigned wr = wrow + sec * 160 + 40 * pp + 32;
  const unsigned wa = wrow + 40 * p + 8 * pp;
  const bool halo = (w == 3 || w == 6), gate = (w == 0 || w == 5);
  const int Tg = 2 * (hg & 3) + p;
  const unsigned xg = (halo ? 8 : Tg) * 2048 + xl;
  const unsigned ga = LDS_GATE + g * 16;

  f32x4 aq[4] = {}, ak[4] = {}, av[4] = {}, ae[5] = {};
  if (halo) kloop<2>(aq, ak, av, ae, xa, wm, wr, wa, xg, ga, voff, gbase, gstep, ldsw);
  else if (gate) kloop<1>(aq, ak, av, ae, xa, wm, wr, wa, xg, ga, voff, gbase, gstep, ldsw);
  else kloop<0>(aq, ak, av, ae, xa, wm, wr, wa, xg, ga, voff, gbase, gstep, ldsw);

  const unsigned lin = EXCH + lane * 8, trw = EXCH + i * 32 + ((g ^ q) << 3);
  if (halo) {
    *(LAS u32x2*)(lin + (24 + h) * 512) = pk4(ae[0]);
    *(LAS u32x2*)(trw + (28 + h) * 512) = pk4(ae[1]);
    *(LAS u32x2*)(lin + (24 + p) * 512) = pk4(ae[2]);
    *(LAS u32x2*)(trw + (28 + p) * 512) = pk4(ae[3]);
    *(LAS u32x2*)((p ? trw : lin) + (32 + p) * 512) = pk4(ae[4]);
  } else {
    const unsigned wb = (sec == 2 ? trw : lin) + (sec * 8 + p * 4) * 512;
#pragma unroll
    for (int t = 0; t < 4; ++t) *(LAS u32x2*)(wb + t * 512) = pk4(ae[t]);
    if (gate && hg < 4 && lane < 16) {
      float gv = 1.f / (1.f + __expf(-ae[4][0]));
      *(float*)(ws + OMAIN_B + OREM_B + (size_t)(rt * 128 + Tg * 16 + i) * 4) = gv;
    }
  }
  u32x2 Q[4], K[5], V[5];
#pragma unroll
  for (int t = 0; t < 4; ++t) { Q[t] = pk4(aq[t]); K[t + 1] = pk4(ak[t]); V[t + 1] = pk4(av[t]); }
  asm volatile("s_waitcnt lgkmcnt(0)\n\ts_barrier" ::: "memory");
  u32x2 Qr[4], Kr[5], Vp[4], Vc[4];
  const unsigned trr = EXCH + (4 * g + q) * 32;
  K[0] = lds_b64(lin + (24 + h) * 512);
  V[0] = lds_tr(trr + ((pp ^ g) << 3) + (28 + h) * 512);
  Kr[0] = lds_b64(lin + 32 * 512);
  const unsigned vr = trr + ((h ^ g) << 3);
#pragma unroll
  for (int t = 0; t < 4; ++t) {
    Qr[t] = lds_b64(g == h ? lin + (p * 4 + t) * 512 : LDS_ZERO);
    Kr[t + 1] = lds_b64(lin + (8 + p * 4 + t) * 512);
    Vp[t] = lds_tr(pp == t ? vr + (t == 0 ? 33 : 16 + p * 4 + t - 1) * 512 : LDS_ZERO);
    Vc[t] = lds_tr(pp == t ? vr + (16 + p * 4 + t) * 512 : LDS_ZERO);
  }
  const float NEG = -1e30f;
  f32x4 bc, bp, b0;
#pragma unroll
  for (int r = 0; r < 4; ++r) {
    int j = 4 * g + r;
    bc[r] = (j <= i && j >= i - 7) ? 0.f : NEG;
    bp[r] = (j >= i + 9) ? 0.f : NEG;
    b0[r] = (rt > 0 && (p ? (j > i && j < 8) : (j >= i + 9))) ? 0.f : NEG;
  }
  const unsigned mc0 = (4 * g <= i ? 0xffffu : 0u) | (4 * g + 1 <= i ? 0xffff0000u : 0u);
  const unsigned mc1 = (4 * g + 2 <= i ? 0xffffu : 0u) | (4 * g + 3 <= i ? 0xffff0000u : 0u);
  f32x4 sc[4];
  float mx[4];
#pragma unroll
  for (int t = 0; t < 4; ++t) {
    bf16x8 qo = cat(Q[t], Qr[t]);
    f32x4 s1 = mfma(cat(K[t + 1], Kr[t + 1]), qo, bc);
    f32x4 s0 = mfma(cat(K[t], Kr[t]), qo, t == 0 ? b0 : bp);
#pragma unroll
    for (int r = 0; r < 4; ++r) sc[t][r] = fmaxf(s0[r], s1[r]);
    mx[t] = fmaxf(fmaxf(sc[t][0], sc[t][1]), fmaxf(sc[t][2], sc[t][3]));
  }
  const float cs = 0.22360679774997896f * 1.4426950408889634f;
  {
    fswap32(mx[0], mx[1]); fswap32(mx[2], mx[3]);
    float a = fmaxf(mx[0], mx[1]), c = fmaxf(mx[2], mx[3]);
    fswap16(a, c);
    float m = fmaxf(a, c) * cs, m2 = m;
    fswap16(m, m2);
    mx[0] = m; mx[1] = m; fswap32(mx[0], mx[1]);
    mx[2] = m2; mx[3] = m2; fswap32(mx[2], mx[3]);
  }
  float sm[4];
#pragma unroll
  for (int t = 0; t < 4; ++t) {
#pragma unroll
    for (int r = 0; r < 4; ++r) sc[t][r] = __builtin_amdgcn_exp2f(__builtin_fmaf(sc[t][r], cs, -mx[t]));
    sm[t] = (sc[t][0] + sc[t][1]) + (sc[t][2] + sc[t][3]);
  }
  {
    fswap32(sm[0], sm[1]); fswap32(sm[2], sm[3]);
    float a = sm[0] + sm[1], c = sm[2] + sm[3];
    fswap16(a, c);
    float m = __builtin_amdgcn_rcpf(a + c), m2 = m;
    fswap16(m, m2);
    sm[0] = m; sm[1] = m; fswap32(sm[0], sm[1]);
    sm[2] = m2; sm[3] = m2; fswap32(sm[2], sm[3]);
  }
  f32x4 orem = {};
  u32x2 O[4];
#pragma unroll
  for (int t = 0; t < 4; ++t) {
    u32x2 pb = pk4(sc[t] * sm[t]);
    u32x2 pc = {pb.x & mc0, pb.y & mc1}, pv = {pb.x & ~mc0, pb.y & ~mc1};
    bf16x8 po = cat(pv, pc);
    f32x4 z = {};
    O[t] = pk4(mfma(cat(V[t], V[t + 1]), po, z));
    orem = mfma(cat(Vp[t], Vc[t]), po, orem);
  }
  char* om = ws + ((size_t)((rt * 8 + p * 4 + (g >> 1)) * 2 + br) * 16 + 2 * hl + (h >> 1)) * 1024 + ((h & 1) * 32 + (lane & 31)) * 16;
#pragma unroll
  for (int m = 0; m < 2; ++m) {
    *(u32x4*)(om + (size_t)m * 2 * 32768) = swap32x2(O[2 * m], O[2 * m + 1]);
  }
  *(u32x2*)(ws + OMAIN_B + ((size_t)(rt * 8 + p * 4 + g) * 16 + hg) * 512 + h * 128 + i * 8) = pk4(orem);
}

DI u32x4 gload16(const void* base, unsigned voff) { u32x4 r; asm volatile("global_load_dwordx4 %0, %1, %2" : "=v"(r) : "v"(voff), "s"(base) : "memory"); return r; }
DI u32x2 gload8(const void* base, unsigned voff) { u32x2 r; asm volatile("global_load_dwordx2 %0, %1, %2" : "=v"(r) : "v"(voff), "s"(base) : "memory"); return r; }
DI float gload4(const void* base, unsigned voff) { float r; asm volatile("global_load_dword %0, %1, %2" : "=v"(r) : "v"(voff), "s"(base) : "memory"); return r; }

DI void oproj_stage(f32x4 (&acc)[5], const bf16x8 (&o)[4], unsigned wa) {
#pragma unroll
  for (int f = 0; f < 4; ++f) {
    bf16x8 wf[5];
#pragma unroll
    for (int n = 0; n < 5; ++n) wf[n] = lds_trfrag(wa + f * 5120 + n * 32, 1280);
#pragma unroll
    for (int n = 0; n < 5; ++n) acc[n] = mfma(wf[n], o[f], acc[n]);
  }
}

__global__ __launch_bounds__(512, 2) void k_oproj(const char* __restrict__ woA, const char* __restrict__ woB, const char* __restrict__ ws,
                                                  char* __restrict__ out) {
  const int tid = threadIdx.x, lane = tid & 63;
  const int w = __builtin_amdgcn_readfirstlane(tid >> 6);
  const int b = blockIdx.x, xcd = b & 7, idx = b >> 3;
  const int R = xcd * 4 + (idx & 3), c = idx >> 2;
  const int rt = R >> 1, br = w >> 2, j = w & 3;
  const int p = j & 1, t = 2 * (R & 1) + (j >> 1), T = 4 * p + t;
  const int g = lane >> 4, i = lane & 15, q = i >> 2, pp = i & 3;
  constexpr int ST = 40960;

  unsigned vo[5], vr[5];
#pragma unroll
  for (int n = 0; n < 5; ++n) {
    int u = (j * 5 + n) * 64 + lane;
    int rho = u / 10, cc = u - rho * 10;
    vo[n] = ((rho >> 4) * 20 + (rho & 15)) * 1280 + cc * 16;
    int head = 8 * (rho >> 5) + 4 * ((rho >> 3) & 1) + 2 * ((rho >> 4) & 1) + ((rho >> 2) & 1);
    vr[n] = (head * 20 + 16 + (rho & 3)) * 1280 + cc * 16;
  }
  const char* wo = (br ? woB : woA) + c * 160;
  const char* om = ws + (size_t)((rt * 8 + T) * 2 + br) * 16384;
  const char* orm = ws + OMAIN_B + ((size_t)(rt * 8 + T) * 16 + 8 * br) * 512;
  const unsigned ldsw = w * 5120;
  u32x4 of[16];
  u32x2 orl[4], orh[4];
  float gv = gload4(ws + OMAIN_B + OREM_B + (size_t)(rt * 8 + T) * 64, i * 4);
#pragma unroll
  for (int s = 0; s < 4; ++s) {
#pragma unroll
    for (int n = 0; n < 5; ++n) dma16(ldsw + s * ST + n * 1024, vo[n], wo + s * 160 * 1280);
#pragma unroll
    for (int f = 0; f < 4; ++f) of[s * 4 + f] = gload16(om + (s * 4 + f) * 1024, lane * 16);
  }
#pragma unroll
  for (int f = 0; f < 4; ++f) { orl[f] = gload8(orm + f * 1024, lane * 8); orh[f] = gload8(orm + f * 1024 + 512, lane * 8); }

  const unsigned wa = br * 20480 + (16 * (g >> 1) + 4 * (g & 1) + q) * 160 + 8 * pp;
  f32x4 acc[5] = {};
  bf16x8 o[4];
#define OWAIT(n, a, b2, c2, d) asm volatile("s_waitcnt vmcnt(" #n ") lgkmcnt(0)\n\ts_barrier" : "+v"(a), "+v"(b2), "+v"(c2), "+v"(d)::"memory")
  OWAIT(35, of[0], of[1], of[2], of[3]);
  asm volatile("" : "+v"(gv));
#pragma unroll
  for (int f = 0; f < 4; ++f) o[f] = __builtin_bit_cast(bf16x8, of[f]);
  oproj_stage(acc, o, wa);
  OWAIT(26, of[4], of[5], of[6], of[7]);
#pragma unroll
  for (int n = 0; n < 5; ++n) dma16(ldsw + n * 1024, vr[n], wo);
#pragma unroll
  for (int f = 0; f < 4; ++f) o[f] = __builtin_bit_cast(bf16x8, of[4 + f]);
  oproj_stage(acc, o, wa + ST);
  OWAIT(22, of[8], of[9], of[10], of[11]);
#pragma unroll
  for (int f = 0; f < 4; ++f) o[f] = __builtin_bit_cast(bf16x8, of[8 + f]);
  oproj_stage(acc, o, wa + 2 * ST);
  OWAIT(13, of[12], of[13], of[14], of[15]);
#pragma unroll
  for (int f = 0; f < 4; ++f) o[f] = __builtin_bit_cast(bf16x8, of[12 + f]);
  oproj_stage(acc, o, wa + 3 * ST);
  asm volatile("s_waitcnt vmcnt(0) lgkmcnt(0)\n\ts_barrier"
               : "+v"(orl[0]), "+v"(orl[1]), "+v"(orl[2]), "+v"(orl[3]), "+v"(orh[0]), "+v"(orh[1]), "+v"(orh[2]), "+v"(orh[3])::"memory");
#pragma unroll
  for (int f = 0; f < 4; ++f) o[f] = cat(orl[f], orh[f]);
  oproj_stage(acc, o, wa);

  const float sc = br ? 1.f - gv : gv;
#pragma unroll
  for (int n = 0; n < 5; ++n) acc[n] *= sc;
  const unsigned ex = ST + j * 5120 + lane * 16;
  char* orow = out + (size_t)(rt * 128 + 32 * t + 2 * i + p) * 1280 + c * 160;
  if (br) {
    *(LAS f32x4*)(ex) = acc[0]; *(LAS f32x4*)(ex + 1024) = acc[1]; *(LAS f32x4*)(ex + 4096) = acc[4];
  } else {
    *(LAS f32x4*)(ex + 2048) = acc[2]; *(LAS f32x4*)(ex + 3072) = acc[3];
  }
  asm volatile("s_waitcnt lgkmcnt(0)\n\ts_barrier" ::: "memory");
  const int n0 = br ? 2 : 0;
  f32x4 a0 = (br ? acc[2] : acc[0]) + *(const LAS f32x4*)(ex + n0 * 1024);
  f32x4 a1 = (br ? acc[3] : acc[1]) + *(const LAS f32x4*)(ex + n0 * 1024 + 1024);
  *(u32x4*)(orow + n0 * 32 + (g & 1) * 32 + (g >> 1) * 16) = swap16x2(pk4(a0), pk4(a1));
  if (!br) {
    f32x4 a4 = acc[4] + *(const LAS f32x4*)(ex + 4096);
    *(u32x2*)(orow + 128 + g * 8) = pk4(a4);
  }
}

extern "C" void kernel_launch(void* const* d_in, const int* in_sizes, int n_in, void* d_out, int out_size, void* d_ws, size_t ws_size,
                              hipStream_t stream) {
  (void)hipFuncSetAttribute((const void*)k_qkv_attn, hipFuncAttributeMaxDynamicSharedMemorySize, LDS1);
  (void)hipFuncSetAttribute((const void*)k_oproj, hipFuncAttributeMaxDynamicSharedMemorySize, LDS2);
  k_qkv_attn<<<256, 512, LDS1, stream>>>((const char*)d_in[0], (const char*)d_in[1], (const char*)d_in[4], (const char*)d_in[6], (char*)d_ws);
  k_oproj<<<256, 512, LDS2, stream>>>((const char*)d_in[2], (const char*)d_in[5], (const char*)d_ws, (char*)d_out);
}
```

```cpp
#define EXPT 0
#include <hip/hip_runtime.h>
#include <stdint.h>

typedef __bf16 bf16x8 __attribute__((ext_vector_type(8)));
typedef __bf16 bf16x2 __attribute__((ext_vector_type(2)));
typedef short s16x4 __attribute__((ext_vector_type(4)));
typedef float f32x4 __attribute__((ext_vector_type(4)));
typedef float f32x2 __attribute__((ext_vector_type(2)));
typedef unsigned u32x2 __attribute__((ext_vector_type(2)));
typedef unsigned u32x4 __attribute__((ext_vector_type(4)));
#define LAS __attribute__((address_space(3)))
#define DI __device__ __forceinline__

constexpr int SLOT = 49152;
constexpr int XB = 18432;
constexpr int WKS = 15360;
constexpr int LDS_GATE = 3 * SLOT;
constexpr int LDS_ZERO = LDS_GATE + 2048;
constexpr int LDS1 = LDS_ZERO + 64;
constexpr int EXCH = SLOT;
constexpr int LDS2 = 163840;

constexpr size_t OMAIN_B = 4u << 20, OREM_B = 1u << 20;

DI bf16x8 lds_b128(unsigned a) { return __builtin_bit_cast(bf16x8, *(const LAS u32x4*)a); }
DI u32x2 lds_b64(unsigned a) { return *(const LAS u32x2*)a; }
DI u32x2 lds_tr(unsigned a) { return __builtin_bit_cast(u32x2, __builtin_amdgcn_ds_read_tr16_b64_v4i16((LAS s16x4*)a)); }
DI bf16x8 cat(u32x2 a, u32x2 b) { u32x4 r = {a.x, a.y, b.x, b.y}; return __builtin_bit_cast(bf16x8, r); }
DI bf16x8 lds_trfrag(unsigned a, int ustride) { return cat(lds_tr(a), lds_tr(a + ustride)); }
DI f32x4 mfma(bf16x8 a, bf16x8 b, f32x4 c) { return __builtin_amdgcn_mfma_f32_16x16x32_bf16(a, b, c, 0, 0, 0); }
DI unsigned pk(float a, float b) { f32x2 v = {a, b}; return __builtin_bit_cast(unsigned, __builtin_convertvector(v, bf16x2)); }
DI u32x2 pk4(f32x4 v) { u32x2 r = {pk(v[0], v[1]), pk(v[2], v[3])}; return r; }
DI void dma16(unsigned lds, unsigned voff, const void* base) {
  asm volatile("s_mov_b32 m0, %0\n\ts_nop 0\n\tglobal_load_lds_dwordx4 %1, %2" ::"s"(lds), "v"(voff), "s"(base) : "memory");
}
#define WAIT_BAR(n) asm volatile("s_waitcnt vmcnt(" #n ") lgkmcnt(0)\n\ts_barrier" ::: "memory")
DI void swap32(unsigned& a, unsigned& b) { auto r = __builtin_amdgcn_permlane32_swap(a, b, false, false); a = r[0]; b = r[1]; }
DI void swap16(unsigned& a, unsigned& b) { auto r = __builtin_amdgcn_permlane16_swap(a, b, false, false); a = r[0]; b = r[1]; }
DI u32x4 swap32x2(u32x2 a, u32x2 b) { unsigned ax = a.x, ay = a.y, bx = b.x, by = b.y; swap32(ax, bx); swap32(ay, by); u32x4 r = {ax, ay, bx, by}; return r; }
DI u32x4 swap16x2(u32x2 a, u32x2 b) { unsigned ax = a.x, ay = a.y, bx = b.x, by = b.y; swap16(ax, bx); swap16(ay, by); u32x4 r = {ax, ay, bx, by}; return r; }
DI void fswap32(float& a, float& b) { unsigned x = __float_as_uint(a), y = __float_as_uint(b); swap32(x, y); a = __uint_as_float(x); b = __uint_as_float(y); }
DI void fswap16(float& a, float& b) { unsigned x = __float_as_uint(a), y = __float_as_uint(b); swap16(x, y); a = __uint_as_float(x); b = __uint_as_float(y); }

#define STAMP(k)
#define STAMP_DECL
#define STAMP_FLUSH(a, b2)
template <int ROLE>
struct Frags {
  bf16x8 x[4], w[3], e[ROLE == 2 ? 4 : (ROLE == 1 ? 3 : 1)];
};

template <int ROLE>
DI void load_frags(Frags<ROLE>& f, unsigned xa, unsigned wm, unsigned wr, unsigned wa, unsigned xg, unsigned ga) {
#pragma unroll
  for (int t = 0; t < 4; ++t) f.x[t] = lds_b128(xa + t * 2048);
#pragma unroll
  for (int s = 0; s < 3; ++s) f.w[s] = lds_trfrag(wm + s * 160, 3840);
  f.e[0] = lds_trfrag(wr, 3840);
  if constexpr (ROLE == 1) { f.e[1] = lds_b128(xg); f.e[2] = lds_b128(ga); }
  if constexpr (ROLE == 2) { f.e[1] = lds_b128(xg); f.e[2] = lds_trfrag(wa + 160, 3840); f.e[3] = lds_trfrag(wa + 320, 3840); }
}
template <int ROLE>
DI void mma_step(const Frags<ROLE>& f, f32x4 (&aq)[4], f32x4 (&ak)[4], f32x4 (&av)[4], f32x4 (&ae)[5]) {
#pragma unroll
  for (int t = 0; t < 4; ++t) {
    aq[t] = mfma(f.w[0], f.x[t], aq[t]);
    ak[t] = mfma(f.w[1], f.x[t], ak[t]);
    av[t] = mfma(f.x[t], f.w[2], av[t]);
    if constexpr (ROLE != 2) ae[t] = mfma(f.e[0], f.x[t], ae[t]);
  }
  if constexpr (ROLE == 1) ae[4] = mfma(f.e[2], f.e[1], ae[4]);
  if constexpr (ROLE == 2) {
    ae[0] = mfma(f.w[1], f.e[1], ae[0]);
    ae[1] = mfma(f.w[2], f.e[1], ae[1]);
    ae[2] = mfma(f.e[2], f.e[1], ae[2]);
    ae[3] = mfma(f.e[3], f.e[1], ae[3]);
    ae[4] = mfma(f.e[0], f.e[1], ae[4]);
  }
}

template <int ROLE>
DI void kloop(f32x4 (&aq)[4], f32x4 (&ak)[4], f32x4 (&av)[4], f32x4 (&ae)[5], unsigned xa, unsigned wm, unsigned wr, unsigned wa,
              unsigned xg, unsigned ga, const unsigned (&voff)[6], const char* gbase, unsigned gstep, unsigned ldsw) {
  Frags<ROLE> f0, f1;
  WAIT_BAR(12);
  load_frags<ROLE>(f0, xa, wm, wr, wa, xg, ga);
  unsigned so = 0;
  for (int s = 0; s < 10; ++s) {
    load_frags<ROLE>(f1, (xa ^ 64) + so, wm + so + WKS, wr + so + WKS, wa + so + WKS, (xg ^ 64) + so, ga + 64);
    mma_step<ROLE>(f0, aq, ak, av, ae);
    __builtin_amdgcn_sched_barrier(0);
    if (s < 9) {
      if (s < 8) WAIT_BAR(6); else WAIT_BAR(0);
      if (s < 7) {
#pragma unroll
        for (int j = 0; j < 6; ++j) dma16(ldsw + so + j * 1024, voff[j], gbase);
        gbase += gstep;
      }
      so = (so == 2 * SLOT) ? 0 : so + SLOT;
      ga += 128;
      load_frags<ROLE>(f0, xa + so, wm + so, wr + so, wa + so, xg + so, ga);
    }
    mma_step<ROLE>(f1, aq, ak, av, ae);
    __builtin_amdgcn_sched_barrier(0);
  }
}

__global__ __launch_bounds__(512, 2) void k_qkv_attn(const char* __restrict__ x, const char* __restrict__ wA, const char* __restrict__ wB,
                                                     const char* __restrict__ gatew, char* __restrict__ ws) {
  STAMP_DECL; STAMP(0);
  const int tid = threadIdx.x, lane = tid & 63;
  const int w = __builtin_amdgcn_readfirstlane(tid >> 6);
  const int b = blockIdx.x, xcd = b & 7, idx = b >> 3;
  const int rt = (xcd >> 2) * 8 + (idx & 7), hg = (xcd & 3) * 4 + (idx >> 3);
  const int br = hg >> 3, hl = hg & 7;
  const int g = lane >> 4, i = lane & 15, q = i >> 2, pp = i & 3;
  const int p = w >> 2, h = w & 3;

  unsigned voff[6];
  const char* gbase;
  unsigned gstep;
  if (w < 3) {
    gbase = x; gstep = 128;
#pragma unroll
    for (int j = 0; j < 6; ++j) {
      int rho = (w * 6 + j) * 8 + (lane >> 3);
      int T = rho >> 4, ii = rho & 15;
      int row = (T < 8) ? rt * 128 + (T & 3) * 32 + 2 * ii + (T >> 2) : rt * 128 - 16 + 2 * (ii & 7) + (ii < 8 ? 1 : 0);
      row = row < 0 ? 0 : row;
      voff[j] = row * 1280 + (((lane & 7) ^ (ii >> 1)) << 4);
    }
  } else {
    gbase = (br ? wB : wA) + hl * 160; gstep = 64 * 3840;
#pragma unroll
    for (int j = 0; j < 6; ++j) {
      int u = ((w - 3) * 6 + j) * 64 + lane;
      int rho = u / 30, c = u - rho * 30;
      int kap = (rho & ~12) | ((rho & 4) << 1) | ((rho & 8) >> 1);
      int sct = c / 10;
      voff[j] = kap * 3840 + sct * 1280 + (c - sct * 10) * 16;
    }
  }
  const unsigned ldsw = w * 6144;
  if (w == 0) {
    dma16(LDS_GATE, lane * 16, gatew);
    dma16(LDS_GATE + 1024, 1024 + (lane & 15) * 16, gatew);
  }
#pragma unroll
  for (int s = 0; s < 3; ++s) {
#pragma unroll
    for (int j = 0; j < 6; ++j) dma16(ldsw + s * SLOT + j * 1024, voff[j], gbase);
    gbase += gstep;
  }
  STAMP(1);
  if (tid < 16) *(LAS unsigned*)(LDS_ZERO + tid * 4) = 0u;

  const int sec = (0x12201102 >> (4 * w)) & 3;
  const unsigned xl = i * 128 + ((g ^ (i >> 1)) << 4);
  const unsigned xa = p * 8192 + xl;
  const unsigned wrow = XB + (16 * (g >> 1) + 4 * (g & 1) + q) * 480;
  const unsigned wm = wrow + 40 * h + 8 * pp;
  const unsigned wr = wrow + sec * 160 + 40 * pp + 32;
  const unsigned wa = wrow + 40 * p + 8 * pp;
  const bool halo = (w == 3 || w == 6), gate = (w == 0 || w == 5);
  const int Tg = 2 * (hg & 3) + p;
  const unsigned xg = (halo ? 8 : Tg) * 2048 + xl;
  const unsigned ga = LDS_GATE + g * 16;

  f32x4 aq[4] = {}, ak[4] = {}, av[4] = {}, ae[5] = {};
  if (halo) kloop<2>(aq, ak, av, ae, xa, wm, wr, wa, xg, ga, voff, gbase, gstep, ldsw);
  else if (gate) kloop<1>(aq, ak, av, ae, xa, wm, wr, wa, xg, ga, voff, gbase, gstep, ldsw);
  else kloop<0>(aq, ak, av, ae, xa, wm, wr, wa, xg, ga, voff, gbase, gstep, ldsw);

  STAMP(2);
  const unsigned lin = EXCH + lane * 8, trw = EXCH + i * 32 + ((g ^ q) << 3);
  if (halo) {
    *(LAS u32x2*)(lin + (24 + h) * 512) = pk4(ae[0]);
    *(LAS u32x2*)(trw + (28 + h) * 512) = pk4(ae[1]);
    *(LAS u32x2*)(lin + (24 + p) * 512) = pk4(ae[2]);
    *(LAS u32x2*)(trw + (28 + p) * 512) = pk4(ae[3]);
    *(LAS u32x2*)((p ? trw : lin) + (32 + p) * 512) = pk4(ae[4]);
  } else {
    const unsigned wb = (sec == 2 ? trw : lin) + (sec * 8 + p * 4) * 512;
#pragma unroll
    for (int t = 0; t < 4; ++t) *(LAS u32x2*)(wb + t * 512) = pk4(ae[t]);
    if (gate && hg < 4 && lane < 16) {
      float gv = 1.f / (1.f + __expf(-ae[4][0]));
      *(float*)(ws + OMAIN_B + OREM_B + (size_t)(rt * 128 + Tg * 16 + i) * 4) = gv;
    }
  }
  u32x2 Q[4], K[5], V[5];
#pragma unroll
  for (int t = 0; t < 4; ++t) { Q[t] = pk4(aq[t]); K[t + 1] = pk4(ak[t]); V[t + 1] = pk4(av[t]); }
  asm volatile("s_waitcnt lgkmcnt(0)\n\ts_barrier" ::: "memory");
  u32x2 Qr[4], Kr[5], Vp[4], Vc[4];
  const unsigned trr = EXCH + (4 * g + q) * 32;
  K[0] = lds_b64(lin + (24 + h) * 512);
  V[0] = lds_tr(trr + ((pp ^ g) << 3) + (28 + h) * 512);
  Kr[0] = lds_b64(lin + 32 * 512);
  const unsigned vr = trr + ((h ^ g) << 3);
#pragma unroll
  for (int t = 0; t < 4; ++t) {
    Qr[t] = lds_b64(g == h ? lin + (p * 4 + t) * 512 : LDS_ZERO);
    Kr[t + 1] = lds_b64(lin + (8 + p * 4 + t) * 512);
    Vp[t] = lds_tr(pp == t ? vr + (t == 0 ? 33 : 16 + p * 4 + t - 1) * 512 : LDS_ZERO);
    Vc[t] = lds_tr(pp == t ? vr + (16 + p * 4 + t) * 512 : LDS_ZERO);
  }
  const float NEG = -1e30f;
  f32x4 bc, bp, b0;
#pragma unroll
  for (int r = 0; r < 4; ++r) {
    int j = 4 * g + r;
    bc[r] = (j <= i && j >= i - 7) ? 0.f : NEG;
    bp[r] = (j >= i + 9) ? 0.f : NEG;
    b0[r] = (rt > 0 && (p ? (j > i && j < 8) : (j >= i + 9))) ? 0.f : NEG;
  }
  const unsigned mc0 = (4 * g <= i ? 0xffffu : 0u) | (4 * g + 1 <= i ? 0xffff0000u : 0u);
  const unsigned mc1 = (4 * g + 2 <= i ? 0xffffu : 0u) | (4 * g + 3 <= i ? 0xffff0000u : 0u);
  f32x4 sc[4];
  float mx[4];
#pragma unroll
  for (int t = 0; t < 4; ++t) {
    bf16x8 qo = cat(Q[t], Qr[t]);
    f32x4 s1 = mfma(cat(K[t + 1], Kr[t + 1]), qo, bc);
    f32x4 s0 = mfma(cat(K[t], Kr[t]), qo, t == 0 ? b0 : bp);
#pragma unroll
    for (int r = 0; r < 4; ++r) sc[t][r] = fmaxf(s0[r], s1[r]);
    mx[t] = fmaxf(fmaxf(sc[t][0], sc[t][1]), fmaxf(sc[t][2], sc[t][3]));
  }
  const float cs = 0.22360679774997896f * 1.4426950408889634f;
  {
    fswap32(mx[0], mx[1]); fswap32(mx[2], mx[3]);
    float a = fmaxf(mx[0], mx[1]), c = fmaxf(mx[2], mx[3]);
    fswap16(a, c);
    float m = fmaxf(a, c) * cs, m2 = m;
    fswap16(m, m2);
    mx[0] = m; mx[1] = m; fswap32(mx[0], mx[1]);
    mx[2] = m2; mx[3] = m2; fswap32(mx[2], mx[3]);
  }
  float sm[4];
#pragma unroll
  for (int t = 0; t < 4; ++t) {
#pragma unroll
    for (int r = 0; r < 4; ++r) sc[t][r] = __builtin_amdgcn_exp2f(__builtin_fmaf(sc[t][r], cs, -mx[t]));
    sm[t] = (sc[t][0] + sc[t][1]) + (sc[t][2] + sc[t][3]);
  }
  {
    fswap32(sm[0], sm[1]); fswap32(sm[2], sm[3]);
    float a = sm[0] + sm[1], c = sm[2] + sm[3];
    fswap16(a, c);
    float m = __builtin_amdgcn_rcpf(a + c), m2 = m;
    fswap16(m, m2);
    sm[0] = m; sm[1] = m; fswap32(sm[0], sm[1]);
    sm[2] = m2; sm[3] = m2; fswap32(sm[2], sm[3]);
  }
  f32x4 orem = {};
  u32x2 O[4];
#pragma unroll
  for (int t = 0; t < 4; ++t) {
    u32x2 pb = pk4(sc[t] * sm[t]);
    u32x2 pc = {pb.x & mc0, pb.y & mc1}, pv = {pb.x & ~mc0, pb.y & ~mc1};
    bf16x8 po = cat(pv, pc);
    f32x4 z = {};
    O[t] = pk4(mfma(cat(V[t], V[t + 1]), po, z));
    orem = mfma(cat(Vp[t], Vc[t]), po, orem);
  }
  char* om = ws + ((size_t)((rt * 8 + p * 4 + (g >> 1)) * 2 + br) * 16 + 2 * hl + (h >> 1)) * 1024 + ((h & 1) * 32 + (lane & 31)) * 16;
#pragma unroll
  for (int m = 0; m < 2; ++m) {
    *(u32x4*)(om + (size_t)m * 2 * 32768) = swap32x2(O[2 * m], O[2 * m + 1]);
  }
  *(u32x2*)(ws + OMAIN_B + ((size_t)(rt * 8 + p * 4 + g) * 16 + hg) * 512 + h * 128 + i * 8) = pk4(orem);
  STAMP(3); STAMP_FLUSH(0, 4);
}

DI u32x4 gload16(const void* base, unsigned voff) { u32x4 r; asm volatile("global_load_dwordx4 %0, %1, %2" : "=v"(r) : "v"(voff), "s"(base) : "memory"); return r; }
DI u32x2 gload8(const void* base, unsigned voff) { u32x2 r; asm volatile("global_load_dwordx2 %0, %1, %2" : "=v"(r) : "v"(voff), "s"(base) : "memory"); return r; }
DI float gload4(const void* base, unsigned voff) { float r; asm volatile("global_load_dword %0, %1, %2" : "=v"(r) : "v"(voff), "s"(base) : "memory"); return r; }

DI void oproj_stage(f32x4 (&acc)[5], const bf16x8 (&o)[4], unsigned wa) {
#pragma unroll
  for (int f = 0; f < 4; ++f) {
    bf16x8 wf[5];
#pragma unroll
    for (int n = 0; n < 5; ++n) wf[n] = lds_trfrag(wa + f * 5120 + n * 32, 1280);
#pragma unroll
    for (int n = 0; n < 5; ++n) acc[n] = mfma(wf[n], o[f], acc[n]);
  }
}

__global__ __launch_bounds__(512, 2) void k_oproj(const char* __restrict__ woA, const char* __restrict__ woB, const char* __restrict__ ws,
                                                  char* __restrict__ out) {
  STAMP_DECL; STAMP(4);
  const int tid = threadIdx.x, lane = tid & 63;
  const int w = __builtin_amdgcn_readfirstlane(tid >> 6);
  const int b = blockIdx.x, xcd = b & 7, idx = b >> 3;
  const int R = xcd * 4 + (idx & 3), c = idx >> 2;
  const int rt = R >> 1, br = w >> 2, j = w & 3;
  const int p = j & 1, t = 2 * (R & 1) + (j >> 1), T = 4 * p + t;
  const int g = lane >> 4, i = lane & 15, q = i >> 2, pp = i & 3;
  constexpr int ST = 40960;

  unsigned vo[5], vr[5];
#pragma unroll
  for (int n = 0; n < 5; ++n) {
    int u = (j * 5 + n) * 64 + lane;
    int rho = u / 10, cc = u - rho * 10;
    vo[n] = ((rho >> 4) * 20 + (rho & 15)) * 1280 + cc * 16;
    int head = 8 * (rho >> 5) + 4 * ((rho >> 3) & 1) + 2 * ((rho >> 4) & 1) + ((rho >> 2) & 1);
    vr[n] = (head * 20 + 16 + (rho & 3)) * 1280 + cc * 16;
  }
  const char* wo = (br ? woB : woA) + c * 160;
  const char* om = ws + (size_t)((rt * 8 + T) * 2 + br) * 16384;
  const char* orm = ws + OMAIN_B + ((size_t)(rt * 8 + T) * 16 + 8 * br) * 512;
  const unsigned ldsw = w * 5120;
  u32x4 of[16];
  u32x2 orl[4], orh[4];
  float gv = gload4(ws + OMAIN_B + OREM_B + (size_t)(rt * 8 + T) * 64, i * 4);
#pragma unroll
  for (int s = 0; s < 4; ++s) {
#pragma unroll
    for (int n = 0; n < 5; ++n) dma16(ldsw + s * ST + n * 1024, vo[n], wo + s * 160 * 1280);
#pragma unroll
    for (int f = 0; f < 4; ++f) of[s * 4 + f] = gload16(om + (s * 4 + f) * 1024, lane * 16);
  }
#pragma unroll
  for (int f = 0; f < 4; ++f) { orl[f] = gload8(orm + f * 1024, lane * 8); orh[f] = gload8(orm + f * 1024 + 512, lane * 8); }

  const unsigned wa = br * 20480 + (16 * (g >> 1) + 4 * (g & 1) + q) * 160 + 8 * pp;
  f32x4 acc[5] = {};
  bf16x8 o[4];
#define OWAIT(n, a, b2, c2, d) asm volatile("s_waitcnt vmcnt(" #n ") lgkmcnt(0)\n\ts_barrier" : "+v"(a), "+v"(b2), "+v"(c2), "+v"(d)::"memory")
  OWAIT(35, of[0], of[1], of[2], of[3]);
  asm volatile("" : "+v"(gv));
  STAMP(5);
#pragma unroll
  for (int f = 0; f < 4; ++f) o[f] = __builtin_bit_cast(bf16x8, of[f]);
  oproj_stage(acc, o, wa);
  OWAIT(26, of[4], of[5], of[6], of[7]);
#pragma unroll
  for (int n = 0; n < 5; ++n) dma16(ldsw + n * 1024, vr[n], wo);
#pragma unroll
  for (int f = 0; f < 4; ++f) o[f] = __builtin_bit_cast(bf16x8, of[4 + f]);
  oproj_stage(acc, o, wa + ST);
  OWAIT(22, of[8], of[9], of[10], of[11]);
#pragma unroll
  for (int f = 0; f < 4; ++f) o[f] = __builtin_bit_cast(bf16x8, of[8 + f]);
  oproj_stage(acc, o, wa + 2 * ST);
  OWAIT(13, of[12], of[13], of[14], of[15]);
#pragma unroll
  for (int f = 0; f < 4; ++f) o[f] = __builtin_bit_cast(bf16x8, of[12 + f]);
  oproj_stage(acc, o, wa + 3 * ST);
  asm volatile("s_waitcnt vmcnt(0) lgkmcnt(0)\n\ts_barrier"
               : "+v"(orl[0]), "+v"(orl[1]), "+v"(orl[2]), "+v"(orl[3]), "+v"(orh[0]), "+v"(orh[1]), "+v"(orh[2]), "+v"(orh[3])::"memory");
#pragma unroll
  for (int f = 0; f < 4; ++f) o[f] = cat(orl[f], orh[f]);
  oproj_stage(acc, o, wa);

  STAMP(6);
  const float sc = br ? 1.f - gv : gv;
#pragma unroll
  for (int n = 0; n < 5; ++n) acc[n] *= sc;
  const unsigned ex = ST + j * 5120 + lane * 16;
  char* orow = out + (size_t)(rt * 128 + 32 * t + 2 * i + p) * 1280 + c * 160;
  if (br) {
    *(LAS f32x4*)(ex) = acc[0]; *(LAS f32x4*)(ex + 1024) = acc[1]; *(LAS f32x4*)(ex + 4096) = acc[4];
  } else {
    *(LAS f32x4*)(ex + 2048) = acc[2]; *(LAS f32x4*)(ex + 3072) = acc[3];
  }
  asm volatile("s_waitcnt lgkmcnt(0)\n\ts_barrier" ::: "memory");
  const int n0 = br ? 2 : 0;
  f32x4 a0 = (br ? acc[2] : acc[0]) + *(const LAS f32x4*)(ex + n0 * 1024);
  f32x4 a1 = (br ? acc[3] : acc[1]) + *(const LAS f32x4*)(ex + n0 * 1024 + 1024);
  *(u32x4*)(orow + n0 * 32 + (g & 1) * 32 + (g >> 1) * 16) = swap16x2(pk4(a0), pk4(a1));
  if (!br) {
    f32x4 a4 = acc[4] + *(const LAS f32x4*)(ex + 4096);
    *(u32x2*)(orow + 128 + g * 8) = pk4(a4);
  }
  STAMP(7); STAMP_FLUSH(4, 8);
}

extern "C" void kernel_launch(void* const* d_in, const int* in_sizes, int n_in, void* d_out, int out_size, void* d_ws, size_t ws_size,
                              hipStream_t stream) {
  (void)hipFuncSetAttribute((const void*)k_qkv_attn, hipFuncAttributeMaxDynamicSharedMemorySize, LDS1);
  (void)hipFuncSetAttribute((const void*)k_oproj, hipFuncAttributeMaxDynamicSharedMemorySize, LDS2);
  k_qkv_attn<<<256, 512, LDS1, stream>>>((const char*)d_in[0], (const char*)d_in[1], (const char*)d_in[4], (const char*)d_in[6], (char*)d_ws);
  k_oproj<<<256, 512, LDS2, stream>>>((const char*)d_in[2], (const char*)d_in[5], (const char*)d_ws, (char*)d_out);
}
```

```cpp
#define EXPT 6
#include <hip/hip_runtime.h>
#include <stdint.h>

typedef __bf16 bf16x8 __attribute__((ext_vector_type(8)));
typedef __bf16 bf16x2 __attribute__((ext_vector_type(2)));
typedef short s16x4 __attribute__((ext_vector_type(4)));
typedef float f32x4 __attribute__((ext_vector_type(4)));
typedef float f32x2 __attribute__((ext_vector_type(2)));
typedef unsigned u32x2 __attribute__((ext_vector_type(2)));
typedef unsigned u32x4 __attribute__((ext_vector_type(4)));
#define LAS __attribute__((address_space(3)))
#define DI __device__ __forceinline__

constexpr int SLOT = 49152;
constexpr int XB = 18432;
constexpr int WKS = 15360;
constexpr int LDS_GATE = 3 * SLOT;
constexpr int LDS_ZERO = LDS_GATE + 2048;
constexpr int LDS1 = LDS_ZERO + 64;
constexpr int EXCH = SLOT;
constexpr int LDS2 = 163840;

constexpr size_t OMAIN_B = 4u << 20, OREM_B = 1u << 20;

DI bf16x8 lds_b128(unsigned a) { return __builtin_bit_cast(bf16x8, *(const LAS u32x4*)a); }
DI u32x2 lds_b64(unsigned a) { return *(const LAS u32x2*)a; }
DI u32x2 lds_tr(unsigned a) { return __builtin_bit_cast(u32x2, __builtin_amdgcn_ds_read_tr16_b64_v4i16((LAS s16x4*)a)); }
DI bf16x8 cat(u32x2 a, u32x2 b) { u32x4 r = {a.x, a.y, b.x, b.y}; return __builtin_bit_cast(bf16x8, r); }
DI bf16x8 lds_trfrag(unsigned a, int ustride) { return cat(lds_tr(a), lds_tr(a + ustride)); }
DI f32x4 mfma(bf16x8 a, bf16x8 b, f32x4 c) { return __builtin_amdgcn_mfma_f32_16x16x32_bf16(a, b, c, 0, 0, 0); }
DI unsigned pk(float a, float b) { f32x2 v = {a, b}; return __builtin_bit_cast(unsigned, __builtin_convertvector(v, bf16x2)); }
DI u32x2 pk4(f32x4 v) { u32x2 r = {pk(v[0], v[1]), pk(v[2], v[3])}; return r; }
DI void dma16(unsigned lds, unsigned voff, const void* base) {
  asm volatile("s_mov_b32 m0, %0\n\ts_nop 0\n\tglobal_load_lds_dwordx4 %1, %2" ::"s"(lds), "v"(voff), "s"(base) : "memory");
}
#define WAIT_BAR(n) asm volatile("s_waitcnt vmcnt(" #n ") lgkmcnt(0)\n\ts_barrier" ::: "memory")
DI void swap32(unsigned& a, unsigned& b) { auto r = __builtin_amdgcn_permlane32_swap(a, b, false, false); a = r[0]; b = r[1]; }
DI void swap16(unsigned& a, unsigned& b) { auto r = __builtin_amdgcn_permlane16_swap(a, b, false, false); a = r[0]; b = r[1]; }
DI u32x4 swap32x2(u32x2 a, u32x2 b) { unsigned ax = a.x, ay = a.y, bx = b.x, by = b.y; swap32(ax, bx); swap32(ay, by); u32x4 r = {ax, ay, bx, by}; return r; }
DI u32x4 swap16x2(u32x2 a, u32x2 b) { unsigned ax = a.x, ay = a.y, bx = b.x, by = b.y; swap16(ax, bx); swap16(ay, by); u32x4 r = {ax, ay, bx, by}; return r; }
DI void fswap32(float& a, float& b) { unsigned x = __float_as_uint(a), y = __float_as_uint(b); swap32(x, y); a = __uint_as_float(x); b = __uint_as_float(y); }
DI void fswap16(float& a, float& b) { unsigned x = __float_as_uint(a), y = __float_as_uint(b); swap16(x, y); a = __uint_as_float(x); b = __uint_as_float(y); }

#define STAMP(k)
#define STAMP_DECL
#define STAMP_FLUSH(a, b2)
template <int ROLE>
struct Frags {
  bf16x8 x[4], w[3], e[ROLE == 2 ? 4 : (ROLE == 1 ? 3 : 1)];
};

template <int ROLE>
DI void load_frags(Frags<ROLE>& f, unsigned xa, unsigned wm, unsigned wr, unsigned wa, unsigned xg, unsigned ga) {
#pragma unroll
  for (int t = 0; t < 4; ++t) f.x[t] = lds_b128(xa + t * 2048);
#pragma unroll
  for (int s = 0; s < 3; ++s) f.w[s] = lds_trfrag(wm + s * 160, 3840);
  f.e[0] = lds_trfrag(wr, 3840);
  if constexpr (ROLE == 1) { f.e[1] = lds_b128(xg); f.e[2] = lds_b128(ga); }
  if constexpr (ROLE == 2) { f.e[1] = lds_b128(xg); f.e[2] = lds_trfrag(wa + 160, 3840); f.e[3] = lds_trfrag(wa + 320, 3840); }
}
template <int ROLE>
DI void mma_step(const Frags<ROLE>& f, f32x4 (&aq)[4], f32x4 (&ak)[4], f32x4 (&av)[4], f32x4 (&ae)[5]) {
#pragma unroll
  for (int t = 0; t < 4; ++t) {
    aq[t] = mfma(f.w[0], f.x[t], aq[t]);
    ak[t] = mfma(f.w[1], f.x[t], ak[t]);
    av[t] = mfma(f.x[t], f.w[2], av[t]);
    if constexpr (ROLE != 2) ae[t] = mfma(f.e[0], f.x[t], ae[t]);
  }
  if constexpr (ROLE == 1) ae[4] = mfma(f.e[2], f.e[1], ae[4]);
  if constexpr (ROLE == 2) {
    ae[0] = mfma(f.w[1], f.e[1], ae[0]);
    ae[1] = mfma(f.w[2], f.e[1], ae[1]);
    ae[2] = mfma(f.e[2], f.e[1], ae[2]);
    ae[3] = mfma(f.e[3], f.e[1], ae[3]);
    ae[4] = mfma(f.e[0], f.e[1], ae[4]);
  }
}

template <int ROLE>
DI void kloop(f32x4 (&aq)[4], f32x4 (&ak)[4], f32x4 (&av)[4], f32x4 (&ae)[5], unsigned xa, unsigned wm, unsigned wr, unsigned wa,
              unsigned xg, unsigned ga, const unsigned (&voff)[6], const char* gbase, unsigned gstep, unsigned ldsw) {
  Frags<ROLE> f0, f1;
  WAIT_BAR(12);
  load_frags<ROLE>(f0, xa, wm, wr, wa, xg, ga);
  unsigned so = 0;
  for (int s = 0; s < 10; ++s) {
    load_frags<ROLE>(f1, (xa ^ 64) + so, wm + so + WKS, wr + so + WKS, wa + so + WKS, (xg ^ 64) + so, ga + 64);
    mma_step<ROLE>(f0, aq, ak, av, ae);
    __builtin_amdgcn_sched_barrier(0);
    if (s < 9) {
      if (s < 8) WAIT_BAR(6); else WAIT_BAR(0);
      if (s < 7) {
#pragma unroll
        for (int j = 0; j < 6; ++j) dma16(ldsw + so + j * 1024, voff[j], gbase);
        gbase += gstep;
      }
      so = (so == 2 * SLOT) ? 0 : so + SLOT;
      ga += 128;
      load_frags<ROLE>(f0, xa + so, wm + so, wr + so, wa + so, xg + so, ga);
    }
    mma_step<ROLE>(f1, aq, ak, av, ae);
    __builtin_amdgcn_sched_barrier(0);
  }
}

__global__ __launch_bounds__(512, 2) void k_qkv_attn(const char* __restrict__ x, const char* __restrict__ wA, const char* __restrict__ wB,
                                                     const char* __restrict__ gatew, char* __restrict__ ws, const char* woA, const char* woB, const char* outp) {
  STAMP_DECL; STAMP(0);
  { unsigned long long t0_ = __builtin_amdgcn_s_memrealtime();
    unsigned acc_ = 0; int id_ = (blockIdx.x >> 3) * 512 + threadIdx.x;
    for (int k_ = id_; k_ < 2621440 / 128; k_ += 16384) acc_ += *(const unsigned*)(x + (size_t)k_ * 128);
    for (int k_ = id_; k_ < 2457600 / 128; k_ += 16384) acc_ += *(const unsigned*)(wA + (size_t)k_ * 128) + *(const unsigned*)(wB + (size_t)k_ * 128);
    for (int k_ = id_; k_ < 819200 / 128; k_ += 16384) acc_ += *(const unsigned*)(woA + (size_t)k_ * 128) + *(const unsigned*)(woB + (size_t)k_ * 128);
    for (int k_ = id_; k_ < 2621440 / 4096; k_ += 16384) acc_ += *(const unsigned*)(outp + (size_t)k_ * 4096);
    for (int k_ = id_; k_ < 5300000 / 4096; k_ += 16384) acc_ += *(const unsigned*)(ws + (size_t)k_ * 4096);
    { unsigned long long pc_; asm volatile("s_getpc_b64 %0" : "=s"(pc_));
      if (id_ < 300) acc_ += *(const unsigned*)((pc_ & ~63ull) + (size_t)id_ * 64); }
    asm volatile("" :: "v"(acc_));
    while (__builtin_amdgcn_s_memrealtime() - t0_ < 1000) __builtin_amdgcn_s_sleep(1); }
  const int tid = threadIdx.x, lane = tid & 63;
  const int w = __builtin_amdgcn_readfirstlane(tid >> 6);
  const int b = blockIdx.x, xcd = b & 7, idx = b >> 3;
  const int rt = (xcd >> 2) * 8 + (idx & 7), hg = (xcd & 3) * 4 + (idx >> 3);
  const int br = hg >> 3, hl = hg & 7;
  const int g = lane >> 4, i = lane & 15, q = i >> 2, pp = i & 3;
  const int p = w >> 2, h = w & 3;

  unsigned voff[6];
  const char* gbase;
  unsigned gstep;
  if (w < 3) {
    gbase = x; gstep = 128;
#pragma unroll
    for (int j = 0; j < 6; ++j) {
      int rho = (w * 6 + j) * 8 + (lane >> 3);
      int T = rho >> 4, ii = rho & 15;
      int row = (T < 8) ? rt * 128 + (T & 3) * 32 + 2 * ii + (T >> 2) : rt * 128 - 16 + 2 * (ii & 7) + (ii < 8 ? 1 : 0);
      row = row < 0 ? 0 : row;
      voff[j] = row * 1280 + (((lane & 7) ^ (ii >> 1)) << 4);
    }
  } else {
    gbase = (br ? wB : wA) + hl * 160; gstep = 64 * 3840;
#pragma unroll
    for (int j = 0; j < 6; ++j) {
      int u = ((w - 3) * 6 + j) * 64 + lane;
      int rho = u / 30, c = u - rho * 30;
      int kap = (rho & ~12) | ((rho & 4) << 1) | ((rho & 8) >> 1);
      int sct = c / 10;
      voff[j] = kap * 3840 + sct * 1280 + (c - sct * 10) * 16;
    }
  }
  const unsigned ldsw = w * 6144;
  if (w == 0) {
    dma16(LDS_GATE, lane * 16, gatew);
    dma16(LDS_GATE + 1024, 1024 + (lane & 15) * 16, gatew);
  }
#pragma unroll
  for (int s = 0; s < 3; ++s) {
#pragma unroll
    for (int j = 0; j < 6; ++j) dma16(ldsw + s * SLOT + j * 1024, voff[j], gbase);
    gbase += gstep;
  }
  STAMP(1);
  if (tid < 16) *(LAS unsigned*)(LDS_ZERO + tid * 4) = 0u;

  const int sec = (0x12201102 >> (4 * w)) & 3;
  const unsigned xl = i * 128 + ((g ^ (i >> 1)) << 4);
  const unsigned xa = p * 8192 + xl;
  const unsigned wrow = XB + (16 * (g >> 1) + 4 * (g & 1) + q) * 480;
  const unsigned wm = wrow + 40 * h + 8 * pp;
  const unsigned wr = wrow + sec * 160 + 40 * pp + 32;
  const unsigned wa = wrow + 40 * p + 8 * pp;
  const bool halo = (w == 3 || w == 6), gate = (w == 0 || w == 5);
  const int Tg = 2 * (hg & 3) + p;
  const unsigned xg = (halo ? 8 : Tg) * 2048 + xl;
  const unsigned ga = LDS_GATE + g * 16;

  f32x4 aq[4] = {}, ak[4] = {}, av[4] = {}, ae[5] = {};
  if (halo) kloop<2>(aq, ak, av, ae, xa, wm, wr, wa, xg, ga, voff, gbase, gstep, ldsw);
  else if (gate) kloop<1>(aq, ak, av, ae, xa, wm, wr, wa, xg, ga, voff, gbase, gstep, ldsw);
  else kloop<0>(aq, ak, av, ae, xa, wm, wr, wa, xg, ga, voff, gbase, gstep, ldsw);

  STAMP(2);
  const unsigned lin = EXCH + lane * 8, trw = EXCH + i * 32 + ((g ^ q) << 3);
  if (halo) {
    *(LAS u32x2*)(lin + (24 + h) * 512) = pk4(ae[0]);
    *(LAS u32x2*)(trw + (28 + h) * 512) = pk4(ae[1]);
    *(LAS u32x2*)(lin + (24 + p) * 512) = pk4(ae[2]);
    *(LAS u32x2*)(trw + (28 + p) * 512) = pk4(ae[3]);
    *(LAS u32x2*)((p ? trw : lin) + (32 + p) * 512) = pk4(ae[4]);
  } else {
    const unsigned wb = (sec == 2 ? trw : lin) + (sec * 8 + p * 4) * 512;
#pragma unroll
    for (int t = 0; t < 4; ++t) *(LAS u32x2*)(wb + t * 512) = pk4(ae[t]);
    if (gate && hg < 4 && lane < 16) {
      float gv = 1.f / (1.f + __expf(-ae[4][0]));
      *(float*)(ws + OMAIN_B + OREM_B + (size_t)(rt * 128 + Tg * 16 + i) * 4) = gv;
    }
  }
  u32x2 Q[4], K[5], V[5];
#pragma unroll
  for (int t = 0; t < 4; ++t) { Q[t] = pk4(aq[t]); K[t + 1] = pk4(ak[t]); V[t + 1] = pk4(av[t]); }
  asm volatile("s_waitcnt lgkmcnt(0)\n\ts_barrier" ::: "memory");
  u32x2 Qr[4], Kr[5], Vp[4], Vc[4];
  const unsigned trr = EXCH + (4 * g + q) * 32;
  K[0] = lds_b64(lin + (24 + h) * 512);
  V[0] = lds_tr(trr + ((pp ^ g) << 3) + (28 + h) * 512);
  Kr[0] = lds_b64(lin + 32 * 512);
  const unsigned vr = trr + ((h ^ g) << 3);
#pragma unroll
  for (int t = 0; t < 4; ++t) {
    Qr[t] = lds_b64(g == h ? lin + (p * 4 + t) * 512 : LDS_ZERO);
    Kr[t + 1] = lds_b64(lin + (8 + p * 4 + t) * 512);
    Vp[t] = lds_tr(pp == t ? vr + (t == 0 ? 33 : 16 + p * 4 + t - 1) * 512 : LDS_ZERO);
    Vc[t] = lds_tr(pp == t ? vr + (16 + p * 4 + t) * 512 : LDS_ZERO);
  }
  const float NEG = -1e30f;
  f32x4 bc, bp, b0;
#pragma unroll
  for (int r = 0; r < 4; ++r) {
    int j = 4 * g + r;
    bc[r] = (j <= i && j >= i - 7) ? 0.f : NEG;
    bp[r] = (j >= i + 9) ? 0.f : NEG;
    b0[r] = (rt > 0 && (p ? (j > i && j < 8) : (j >= i + 9))) ? 0.f : NEG;
  }
  const unsigned mc0 = (4 * g <= i ? 0xffffu : 0u) | (4 * g + 1 <= i ? 0xffff0000u : 0u);
  const unsigned mc1 = (4 * g + 2 <= i ? 0xffffu : 0u) | (4 * g + 3 <= i ? 0xffff0000u : 0u);
  f32x4 sc[4];
  float mx[4];
#pragma unroll
  for (int t = 0; t < 4; ++t) {
    bf16x8 qo = cat(Q[t], Qr[t]);
    f32x4 s1 = mfma(cat(K[t + 1], Kr[t + 1]), qo, bc);
    f32x4 s0 = mfma(cat(K[t], Kr[t]), qo, t == 0 ? b0 : bp);
#pragma unroll
    for (int r = 0; r < 4; ++r) sc[t][r] = fmaxf(s0[r], s1[r]);
    mx[t] = fmaxf(fmaxf(sc[t][0], sc[t][1]), fmaxf(sc[t][2], sc[t][3]));
  }
  const float cs = 0.22360679774997896f * 1.4426950408889634f;
  {
    fswap32(mx[0], mx[1]); fswap32(mx[2], mx[3]);
    float a = fmaxf(mx[0], mx[1]), c = fmaxf(mx[2], mx[3]);
    fswap16(a, c);
    float m = fmaxf(a, c) * cs, m2 = m;
    fswap16(m, m2);
    mx[0] = m; mx[1] = m; fswap32(mx[0], mx[1]);
    mx[2] = m2; mx[3] = m2; fswap32(mx[2], mx[3]);
  }
  float sm[4];
#pragma unroll
  for (int t = 0; t < 4; ++t) {
#pragma unroll
    for (int r = 0; r < 4; ++r) sc[t][r] = __builtin_amdgcn_exp2f(__builtin_fmaf(sc[t][r], cs, -mx[t]));
    sm[t] = (sc[t][0] + sc[t][1]) + (sc[t][2] + sc[t][3]);
  }
  {
    fswap32(sm[0], sm[1]); fswap32(sm[2], sm[3]);
    float a = sm[0] + sm[1], c = sm[2] + sm[3];
    fswap16(a, c);
    float m = __builtin_amdgcn_rcpf(a + c), m2 = m;
    fswap16(m, m2);
    sm[0] = m; sm[1] = m; fswap32(sm[0], sm[1]);
    sm[2] = m2; sm[3] = m2; fswap32(sm[2], sm[3]);
  }
  f32x4 orem = {};
  u32x2 O[4];
#pragma unroll
  for (int t = 0; t < 4; ++t) {
    u32x2 pb = pk4(sc[t] * sm[t]);
    u32x2 pc = {pb.x & mc0, pb.y & mc1}, pv = {pb.x & ~mc0, pb.y & ~mc1};
    bf16x8 po = cat(pv, pc);
    f32x4 z = {};
    O[t] = pk4(mfma(cat(V[t], V[t + 1]), po, z));
    orem = mfma(cat(Vp[t], Vc[t]), po, orem);
  }
  char* om = ws + ((size_t)((rt * 8 + p * 4 + (g >> 1)) * 2 + br) * 16 + 2 * hl + (h >> 1)) * 1024 + ((h & 1) * 32 + (lane & 31)) * 16;
#pragma unroll
  for (int m = 0; m < 2; ++m) {
    *(u32x4*)(om + (size_t)m * 2 * 32768) = swap32x2(O[2 * m], O[2 * m + 1]);
  }
  *(u32x2*)(ws + OMAIN_B + ((size_t)(rt * 8 + p * 4 + g) * 16 + hg) * 512 + h * 128 + i * 8) = pk4(orem);
  STAMP(3); STAMP_FLUSH(0, 4);
}

DI u32x4 gload16(const void* base, unsigned voff) { u32x4 r; asm volatile("global_load_dwordx4 %0, %1, %2" : "=v"(r) : "v"(voff), "s"(base) : "memory"); return r; }
DI u32x2 gload8(const void* base, unsigned voff) { u32x2 r; asm volatile("global_load_dwordx2 %0, %1, %2" : "=v"(r) : "v"(voff), "s"(base) : "memory"); return r; }
DI float gload4(const void* base, unsigned voff) { float r; asm volatile("global_load_dword %0, %1, %2" : "=v"(r) : "v"(voff), "s"(base) : "memory"); return r; }

DI void oproj_stage(f32x4 (&acc)[5], const bf16x8 (&o)[4], unsigned wa) {
#pragma unroll
  for (int f = 0; f < 4; ++f) {
    bf16x8 wf[5];
#pragma unroll
    for (int n = 0; n < 5; ++n) wf[n] = lds_trfrag(wa + f * 5120 + n * 32, 1280);
#pragma unroll
    for (int n = 0; n < 5; ++n) acc[n] = mfma(wf[n], o[f], acc[n]);
  }
}

__global__ __launch_bounds__(512, 2) void k_oproj(const char* __restrict__ woA, const char* __restrict__ woB, const char* __restrict__ ws,
                                                  char* __restrict__ out) {
  STAMP_DECL; STAMP(4);
  const int tid = threadIdx.x, lane = tid & 63;
  const int w = __builtin_amdgcn_readfirstlane(tid >> 6);
  const int b = blockIdx.x, xcd = b & 7, idx = b >> 3;
  const int R = xcd * 4 + (idx & 3), c = idx >> 2;
  const int rt = R >> 1, br = w >> 2, j = w & 3;
  const int p = j & 1, t = 2 * (R & 1) + (j >> 1), T = 4 * p + t;
  const int g = lane >> 4, i = lane & 15, q = i >> 2, pp = i & 3;
  constexpr int ST = 40960;

  unsigned vo[5], vr[5];
#pragma unroll
  for (int n = 0; n < 5; ++n) {
    int u = (j * 5 + n) * 64 + lane;
    int rho = u / 10, cc = u - rho * 10;
    vo[n] = ((rho >> 4) * 20 + (rho & 15)) * 1280 + cc * 16;
    int head = 8 * (rho >> 5) + 4 * ((rho >> 3) & 1) + 2 * ((rho >> 4) & 1) + ((rho >> 2) & 1);
    vr[n] = (head * 20 + 16 + (rho & 3)) * 1280 + cc * 16;
  }
  const char* wo = (br ? woB : woA) + c * 160;
  const char* om = ws + (size_t)((rt * 8 + T) * 2 + br) * 16384;
  const char* orm = ws + OMAIN_B + ((size_t)(rt * 8 + T) * 16 + 8 * br) * 512;
  const unsigned ldsw = w * 5120;
  u32x4 of[16];
  u32x2 orl[4], orh[4];
  float gv = gload4(ws + OMAIN_B + OREM_B + (size_t)(rt * 8 + T) * 64, i * 4);
#pragma unroll
  for (int s = 0; s < 4; ++s) {
#pragma unroll
    for (int n = 0; n < 5; ++n) dma16(ldsw + s * ST + n * 1024, vo[n], wo + s * 160 * 1280);
#pragma unroll
    for (int f = 0; f < 4; ++f) of[s * 4 + f] = gload16(om + (s * 4 + f) * 1024, lane * 16);
  }
#pragma unroll
  for (int f = 0; f < 4; ++f) { orl[f] = gload8(orm + f * 1024, lane * 8); orh[f] = gload8(orm + f * 1024 + 512, lane * 8); }

  const unsigned wa = br * 20480 + (16 * (g >> 1) + 4 * (g & 1) + q) * 160 + 8 * pp;
  f32x4 acc[5] = {};
  bf16x8 o[4];
#define OWAIT(n, a, b2, c2, d) asm volatile("s_waitcnt vmcnt(" #n ") lgkmcnt(0)\n\ts_barrier" : "+v"(a), "+v"(b2), "+v"(c2), "+v"(d)::"memory")
  OWAIT(35, of[0], of[1], of[2], of[3]);
  asm volatile("" : "+v"(gv));
  STAMP(5);
#pragma unroll
  for (int f = 0; f < 4; ++f) o[f] = __builtin_bit_cast(bf16x8, of[f]);
  oproj_stage(acc, o, wa);
  OWAIT(26, of[4], of[5], of[6], of[7]);
#pragma unroll
  for (int n = 0; n < 5; ++n) dma16(ldsw + n * 1024, vr[n], wo);
#pragma unroll
  for (int f = 0; f < 4; ++f) o[f] = __builtin_bit_cast(bf16x8, of[4 + f]);
  oproj_stage(acc, o, wa + ST);
  OWAIT(22, of[8], of[9], of[10], of[11]);
#pragma unroll
  for (int f = 0; f < 4; ++f) o[f] = __builtin_bit_cast(bf16x8, of[8 + f]);
  oproj_stage(acc, o, wa + 2 * ST);
  OWAIT(13, of[12], of[13], of[14], of[15]);
#pragma unroll
  for (int f = 0; f < 4; ++f) o[f] = __builtin_bit_cast(bf16x8, of[12 + f]);
  oproj_stage(acc, o, wa + 3 * ST);
  asm volatile("s_waitcnt vmcnt(0) lgkmcnt(0)\n\ts_barrier"
               : "+v"(orl[0]), "+v"(orl[1]), "+v"(orl[2]), "+v"(orl[3]), "+v"(orh[0]), "+v"(orh[1]), "+v"(orh[2]), "+v"(orh[3])::"memory");
#pragma unroll
  for (int f = 0; f < 4; ++f) o[f] = cat(orl[f], orh[f]);
  oproj_stage(acc, o, wa);

  STAMP(6);
  const float sc = br ? 1.f - gv : gv;
#pragma unroll
  for (int n = 0; n < 5; ++n) acc[n] *= sc;
  const unsigned ex = ST + j * 5120 + lane * 16;
  char* orow = out + (size_t)(rt * 128 + 32 * t + 2 * i + p) * 1280 + c * 160;
  if (br) {
    *(LAS f32x4*)(ex) = acc[0]; *(LAS f32x4*)(ex + 1024) = acc[1]; *(LAS f32x4*)(ex + 4096) = acc[4];
  } else {
    *(LAS f32x4*)(ex + 2048) = acc[2]; *(LAS f32x4*)(ex + 3072) = acc[3];
  }
  asm volatile("s_waitcnt lgkmcnt(0)\n\ts_barrier" ::: "memory");
  const int n0 = br ? 2 : 0;
  f32x4 a0 = (br ? acc[2] : acc[0]) + *(const LAS f32x4*)(ex + n0 * 1024);
  f32x4 a1 = (br ? acc[3] : acc[1]) + *(const LAS f32x4*)(ex + n0 * 1024 + 1024);
  *(u32x4*)(orow + n0 * 32 + (g & 1) * 32 + (g >> 1) * 16) = swap16x2(pk4(a0), pk4(a1));
  if (!br) {
    f32x4 a4 = acc[4] + *(const LAS f32x4*)(ex + 4096);
    *(u32x2*)(orow + 128 + g * 8) = pk4(a4);
  }
  STAMP(7); STAMP_FLUSH(4, 8);
}

extern "C" void kernel_launch(void* const* d_in, const int* in_sizes, int n_in, void* d_out, int out_size, void* d_ws, size_t ws_size,
                              hipStream_t stream) {
  (void)hipFuncSetAttribute((const void*)k_qkv_attn, hipFuncAttributeMaxDynamicSharedMemorySize, LDS1);
  (void)hipFuncSetAttribute((const void*)k_oproj, hipFuncAttributeMaxDynamicSharedMemorySize, LDS2);
  k_qkv_attn<<<256, 512, LDS1, stream>>>((const char*)d_in[0], (const char*)d_in[1], (const char*)d_in[4], (const char*)d_in[6], (char*)d_ws, (const char*)d_in[2], (const char*)d_in[5], (const char*)d_out);
  k_oproj<<<256, 512, LDS2, stream>>>((const char*)d_in[2], (const char*)d_in[5], (const char*)d_ws, (char*)d_out);
}
```

```cpp
#define EXPT 4
#include <hip/hip_runtime.h>
#include <stdint.h>

typedef __bf16 bf16x8 __attribute__((ext_vector_type(8)));
typedef __bf16 bf16x2 __attribute__((ext_vector_type(2)));
typedef short s16x4 __attribute__((ext_vector_type(4)));
typedef float f32x4 __attribute__((ext_vector_type(4)));
typedef float f32x2 __attribute__((ext_vector_type(2)));
typedef unsigned u32x2 __attribute__((ext_vector_type(2)));
typedef unsigned u32x4 __attribute__((ext_vector_type(4)));
#define LAS __attribute__((address_space(3)))
#define DI __device__ __forceinline__

constexpr int SLOT = 49152;
constexpr int XB = 18432;
constexpr int WKS = 15360;
constexpr int LDS_GATE = 3 * SLOT;
constexpr int LDS_ZERO = LDS_GATE + 2048;
constexpr int LDS1 = LDS_ZERO + 64;
constexpr int EXCH = SLOT;
constexpr int LDS2 = 163840;

constexpr size_t OMAIN_B = 4u << 20, OREM_B = 1u << 20;

DI bf16x8 lds_b128(unsigned a) { return __builtin_bit_cast(bf16x8, *(const LAS u32x4*)a); }
DI u32x2 lds_b64(unsigned a) { return *(const LAS u32x2*)a; }
DI u32x2 lds_tr(unsigned a) { return __builtin_bit_cast(u32x2, __builtin_amdgcn_ds_read_tr16_b64_v4i16((LAS s16x4*)a)); }
DI bf16x8 cat(u32x2 a, u32x2 b) { u32x4 r = {a.x, a.y, b.x, b.y}; return __builtin_bit_cast(bf16x8, r); }
DI bf16x8 lds_trfrag(unsigned a, int ustride) { return cat(lds_tr(a), lds_tr(a + ustride)); }
DI f32x4 mfma(bf16x8 a, bf16x8 b, f32x4 c) { return __builtin_amdgcn_mfma_f32_16x16x32_bf16(a, b, c, 0, 0, 0); }
DI unsigned pk(float a, float b) { f32x2 v = {a, b}; return __builtin_bit_cast(unsigned, __builtin_convertvector(v, bf16x2)); }
DI u32x2 pk4(f32x4 v) { u32x2 r = {pk(v[0], v[1]), pk(v[2], v[3])}; return r; }
DI void dma16(unsigned lds, unsigned voff, const void* base) {
  asm volatile("s_mov_b32 m0, %0\n\ts_nop 0\n\tglobal_load_lds_dwordx4 %1, %2" ::"s"(lds), "v"(voff), "s"(base) : "memory");
}
#define WAIT_BAR(n) asm volatile("s_waitcnt vmcnt(" #n ") lgkmcnt(0)\n\ts_barrier" ::: "memory")
DI void swap32(unsigned& a, unsigned& b) { auto r = __builtin_amdgcn_permlane32_swap(a, b, false, false); a = r[0]; b = r[1]; }
DI void swap16(unsigned& a, unsigned& b) { auto r = __builtin_amdgcn_permlane16_swap(a, b, false, false); a = r[0]; b = r[1]; }
DI u32x4 swap32x2(u32x2 a, u32x2 b) { unsigned ax = a.x, ay = a.y, bx = b.x, by = b.y; swap32(ax, bx); swap32(ay, by); u32x4 r = {ax, ay, bx, by}; return r; }
DI u32x4 swap16x2(u32x2 a, u32x2 b) { unsigned ax = a.x, ay = a.y, bx = b.x, by = b.y; swap16(ax, bx); swap16(ay, by); u32x4 r = {ax, ay, bx, by}; return r; }
DI void fswap32(float& a, float& b) { unsigned x = __float_as_uint(a), y = __float_as_uint(b); swap32(x, y); a = __uint_as_float(x); b = __uint_as_float(y); }
DI void fswap16(float& a, float& b) { unsigned x = __float_as_uint(a), y = __float_as_uint(b); swap16(x, y); a = __uint_as_float(x); b = __uint_as_float(y); }

#define STAMP(k)
#define STAMP_DECL
#define STAMP_FLUSH(a, b2)
#define UNTIL(e, us) do { if (EXPT == e) { while (__builtin_amdgcn_s_memrealtime() - tstart_ < (us) * 100) __builtin_amdgcn_s_sleep(1); } } while (0)
template <int ROLE>
struct Frags {
  bf16x8 x[4], w[3], e[ROLE == 2 ? 4 : (ROLE == 1 ? 3 : 1)];
};

template <int ROLE>
DI void load_frags(Frags<ROLE>& f, unsigned xa, unsigned wm, unsigned wr, unsigned wa, unsigned xg, unsigned ga) {
#pragma unroll
  for (int t = 0; t < 4; ++t) f.x[t] = lds_b128(xa + t * 2048);
#pragma unroll
  for (int s = 0; s < 3; ++s) f.w[s] = lds_trfrag(wm + s * 160, 3840);
  f.e[0] = lds_trfrag(wr, 3840);
  if constexpr (ROLE == 1) { f.e[1] = lds_b128(xg); f.e[2] = lds_b128(ga); }
  if constexpr (ROLE == 2) { f.e[1] = lds_b128(xg); f.e[2] = lds_trfrag(wa + 160, 3840); f.e[3] = lds_trfrag(wa + 320, 3840); }
}
template <int ROLE>
DI void mma_step(const Frags<ROLE>& f, f32x4 (&aq)[4], f32x4 (&ak)[4], f32x4 (&av)[4], f32x4 (&ae)[5]) {
#pragma unroll
  for (int t = 0; t < 4; ++t) {
    aq[t] = mfma(f.w[0], f.x[t], aq[t]);
    ak[t] = mfma(f.w[1], f.x[t], ak[t]);
    av[t] = mfma(f.x[t], f.w[2], av[t]);
    if constexpr (ROLE != 2) ae[t] = mfma(f.e[0], f.x[t], ae[t]);
  }
  if constexpr (ROLE == 1) ae[4] = mfma(f.e[2], f.e[1], ae[4]);
  if constexpr (ROLE == 2) {
    ae[0] = mfma(f.w[1], f.e[1], ae[0]);
    ae[1] = mfma(f.w[2], f.e[1], ae[1]);
    ae[2] = mfma(f.e[2], f.e[1], ae[2]);
    ae[3] = mfma(f.e[3], f.e[1], ae[3]);
    ae[4] = mfma(f.e[0], f.e[1], ae[4]);
  }
}

template <int ROLE>
DI void kloop(f32x4 (&aq)[4], f32x4 (&ak)[4], f32x4 (&av)[4], f32x4 (&ae)[5], unsigned xa, unsigned wm, unsigned wr, unsigned wa,
              unsigned xg, unsigned ga, const unsigned (&voff)[6], const char* gbase, unsigned gstep, unsigned ldsw) {
  Frags<ROLE> f0, f1;
  WAIT_BAR(12);
  load_frags<ROLE>(f0, xa, wm, wr, wa, xg, ga);
  unsigned so = 0;
  for (int s = 0; s < 10; ++s) {
    load_frags<ROLE>(f1, (xa ^ 64) + so, wm + so + WKS, wr + so + WKS, wa + so + WKS, (xg ^ 64) + so, ga + 64);
    mma_step<ROLE>(f0, aq, ak, av, ae);
    __builtin_amdgcn_sched_barrier(0);
    if (s < 9) {
      if (s < 8) WAIT_BAR(6); else WAIT_BAR(0);
      if (s < 7) {
#pragma unroll
        for (int j = 0; j < 6; ++j) dma16(ldsw + so + j * 1024, voff[j], gbase);
        gbase += gstep;
      }
      so = (so == 2 * SLOT) ? 0 : so + SLOT;
      ga += 128;
      load_frags<ROLE>(f0, xa + so, wm + so, wr + so, wa + so, xg + so, ga);
    }
    mma_step<ROLE>(f1, aq, ak, av, ae);
    __builtin_amdgcn_sched_barrier(0);
  }
}

__global__ __launch_bounds__(512, 2) void k_qkv_attn(const char* __restrict__ x, const char* __restrict__ wA, const char* __restrict__ wB,
                                                     const char* __restrict__ gatew, char* __restrict__ ws) {
  const unsigned long long tstart_ = __builtin_amdgcn_s_memrealtime();
  const int tid = threadIdx.x, lane = tid & 63;
  const int w = __builtin_amdgcn_readfirstlane(tid >> 6);
  const int b = blockIdx.x, xcd = b & 7, idx = b >> 3;
  const int rt = (xcd >> 2) * 8 + (idx & 7), hg = (xcd & 3) * 4 + (idx >> 3);
  const int br = hg >> 3, hl = hg & 7;
  const int g = lane >> 4, i = lane & 15, q = i >> 2, pp = i & 3;
  const int p = w >> 2, h = w & 3;

  unsigned voff[6];
  const char* gbase;
  unsigned gstep;
  if (w < 3) {
    gbase = x; gstep = 128;
#pragma unroll
    for (int j = 0; j < 6; ++j) {
      int rho = (w * 6 + j) * 8 + (lane >> 3);
      int T = rho >> 4, ii = rho & 15;
      int row = (T < 8) ? rt * 128 + (T & 3) * 32 + 2 * ii + (T >> 2) : rt * 128 - 16 + 2 * (ii & 7) + (ii < 8 ? 1 : 0);
      row = row < 0 ? 0 : row;
      voff[j] = row * 1280 + (((lane & 7) ^ (ii >> 1)) << 4);
    }
  } else {
    gbase = (br ? wB : wA) + hl * 160; gstep = 64 * 3840;
#pragma unroll
    for (int j = 0; j < 6; ++j) {
      int u = ((w - 3) * 6 + j) * 64 + lane;
      int rho = u / 30, c = u - rho * 30;
      int kap = (rho & ~12) | ((rho & 4) << 1) | ((rho & 8) >> 1);
      int sct = c / 10;
      voff[j] = kap * 3840 + sct * 1280 + (c - sct * 10) * 16;
    }
  }
  const unsigned ldsw = w * 6144;
  if (w == 0) {
    dma16(LDS_GATE, lane * 16, gatew);
    dma16(LDS_GATE + 1024, 1024 + (lane & 15) * 16, gatew);
  }
#pragma unroll
  for (int s = 0; s < 3; ++s) {
#pragma unroll
    for (int j = 0; j < 6; ++j) dma16(ldsw + s * SLOT + j * 1024, voff[j], gbase);
    gbase += gstep;
  }
  if (EXPT == 5) { asm volatile("s_waitcnt vmcnt(12)" ::: "memory"); } UNTIL(5, 10);
  if (tid < 16) *(LAS unsigned*)(LDS_ZERO + tid * 4) = 0u;

  const int sec = (0x12201102 >> (4 * w)) & 3;
  const unsigned xl = i * 128 + ((g ^ (i >> 1)) << 4);
  const unsigned xa = p * 8192 + xl;
  const unsigned wrow = XB + (16 * (g >> 1) + 4 * (g & 1) + q) * 480;
  const unsigned wm = wrow + 40 * h + 8 * pp;
  const unsigned wr = wrow + sec * 160 + 40 * pp + 32;
  const unsigned wa = wrow + 40 * p + 8 * pp;
  const bool halo = (w == 3 || w == 6), gate = (w == 0 || w == 5);
  const int Tg = 2 * (hg & 3) + p;
  const unsigned xg = (halo ? 8 : Tg) * 2048 + xl;
  const unsigned ga = LDS_GATE + g * 16;

  f32x4 aq[4] = {}, ak[4] = {}, av[4] = {}, ae[5] = {};
  if (halo) kloop<2>(aq, ak, av, ae, xa, wm, wr, wa, xg, ga, voff, gbase, gstep, ldsw);
  else if (gate) kloop<1>(aq, ak, av, ae, xa, wm, wr, wa, xg, ga, voff, gbase, gstep, ldsw);
  else kloop<0>(aq, ak, av, ae, xa, wm, wr, wa, xg, ga, voff, gbase, gstep, ldsw);

  UNTIL(4, 20);
  const unsigned lin = EXCH + lane * 8, trw = EXCH + i * 32 + ((g ^ q) << 3);
  if (halo) {
    *(LAS u32x2*)(lin + (24 + h) * 512) = pk4(ae[0]);
    *(LAS u32x2*)(trw + (28 + h) * 512) = pk4(ae[1]);
    *(LAS u32x2*)(lin + (24 + p) * 512) = pk4(ae[2]);
    *(LAS u32x2*)(trw + (28 + p) * 512) = pk4(ae[3]);
    *(LAS u32x2*)((p ? trw : lin) + (32 + p) * 512) = pk4(ae[4]);
  } else {
    const unsigned wb = (sec == 2 ? trw : lin) + (sec * 8 + p * 4) * 512;
#pragma unroll
    for (int t = 0; t < 4; ++t) *(LAS u32x2*)(wb + t * 512) = pk4(ae[t]);
    if (gate && hg < 4 && lane < 16) {
      float gv = 1.f / (1.f + __expf(-ae[4][0]));
      *(float*)(ws + OMAIN_B + OREM_B + (size_t)(rt * 128 + Tg * 16 + i) * 4) = gv;
    }
  }
  u32x2 Q[4], K[5], V[5];
#pragma unroll
  for (int t = 0; t < 4; ++t) { Q[t] = pk4(aq[t]); K[t + 1] = pk4(ak[t]); V[t + 1] = pk4(av[t]); }
  asm volatile("s_waitcnt lgkmcnt(0)\n\ts_barrier" ::: "memory");
  u32x2 Qr[4], Kr[5], Vp[4], Vc[4];
  const unsigned trr = EXCH + (4 * g + q) * 32;
  K[0] = lds_b64(lin + (24 + h) * 512);
  V[0] = lds_tr(trr + ((pp ^ g) << 3) + (28 + h) * 512);
  Kr[0] = lds_b64(lin + 32 * 512);
  const unsigned vr = trr + ((h ^ g) << 3);
#pragma unroll
  for (int t = 0; t < 4; ++t) {
    Qr[t] = lds_b64(g == h ? lin + (p * 4 + t) * 512 : LDS_ZERO);
    Kr[t + 1] = lds_b64(lin + (8 + p * 4 + t) * 512);
    Vp[t] = lds_tr(pp == t ? vr + (t == 0 ? 33 : 16 + p * 4 + t - 1) * 512 : LDS_ZERO);
    Vc[t] = lds_tr(pp == t ? vr + (16 + p * 4 + t) * 512 : LDS_ZERO);
  }
  const float NEG = -1e30f;
  f32x4 bc, bp, b0;
#pragma unroll
  for (int r = 0; r < 4; ++r) {
    int j = 4 * g + r;
    bc[r] = (j <= i && j >= i - 7) ? 0.f : NEG;
    bp[r] = (j >= i + 9) ? 0.f : NEG;
    b0[r] = (rt > 0 && (p ? (j > i && j < 8) : (j >= i + 9))) ? 0.f : NEG;
  }
  const unsigned mc0 = (4 * g <= i ? 0xffffu : 0u) | (4 * g + 1 <= i ? 0xffff0000u : 0u);
  const unsigned mc1 = (4 * g + 2 <= i ? 0xffffu : 0u) | (4 * g + 3 <= i ? 0xffff0000u : 0u);
  f32x4 sc[4];
  float mx[4];
#pragma unroll
  for (int t = 0; t < 4; ++t) {
    bf16x8 qo = cat(Q[t], Qr[t]);
    f32x4 s1 = mfma(cat(K[t + 1], Kr[t + 1]), qo, bc);
    f32x4 s0 = mfma(cat(K[t], Kr[t]), qo, t == 0 ? b0 : bp);
#pragma unroll
    for (int r = 0; r < 4; ++r) sc[t][r] = fmaxf(s0[r], s1[r]);
    mx[t] = fmaxf(fmaxf(sc[t][0], sc[t][1]), fmaxf(sc[t][2], sc[t][3]));
  }
  const float cs = 0.22360679774997896f * 1.4426950408889634f;
  {
    fswap32(mx[0], mx[1]); fswap32(mx[2], mx[3]);
    float a = fmaxf(mx[0], mx[1]), c = fmaxf(mx[2], mx[3]);
    fswap16(a, c);
    float m = fmaxf(a, c) * cs, m2 = m;
    fswap16(m, m2);
    mx[0] = m; mx[1] = m; fswap32(mx[0], mx[1]);
    mx[2] = m2; mx[3] = m2; fswap32(mx[2], mx[3]);
  }
  float sm[4];
#pragma unroll
  for (int t = 0; t < 4; ++t) {
#pragma unroll
    for (int r = 0; r < 4; ++r) sc[t][r] = __builtin_amdgcn_exp2f(__builtin_fmaf(sc[t][r], cs, -mx[t]));
    sm[t] = (sc[t][0] + sc[t][1]) + (sc[t][2] + sc[t][3]);
  }
  {
    fswap32(sm[0], sm[1]); fswap32(sm[2], sm[3]);
    float a = sm[0] + sm[1], c = sm[2] + sm[3];
    fswap16(a, c);
    float m = __builtin_amdgcn_rcpf(a + c), m2 = m;
    fswap16(m, m2);
    sm[0] = m; sm[1] = m; fswap32(sm[0], sm[1]);
    sm[2] = m2; sm[3] = m2; fswap32(sm[2], sm[3]);
  }
  f32x4 orem = {};
  u32x2 O[4];
#pragma unroll
  for (int t = 0; t < 4; ++t) {
    u32x2 pb = pk4(sc[t] * sm[t]);
    u32x2 pc = {pb.x & mc0, pb.y & mc1}, pv = {pb.x & ~mc0, pb.y & ~mc1};
    bf16x8 po = cat(pv, pc);
    f32x4 z = {};
    O[t] = pk4(mfma(cat(V[t], V[t + 1]), po, z));
    orem = mfma(cat(Vp[t], Vc[t]), po, orem);
  }
  char* om = ws + ((size_t)((rt * 8 + p * 4 + (g >> 1)) * 2 + br) * 16 + 2 * hl + (h >> 1)) * 1024 + ((h & 1) * 32 + (lane & 31)) * 16;
#pragma unroll
  for (int m = 0; m < 2; ++m) {
    *(u32x4*)(om + (size_t)m * 2 * 32768) = swap32x2(O[2 * m], O[2 * m + 1]);
  }
  *(u32x2*)(ws + OMAIN_B + ((size_t)(rt * 8 + p * 4 + g) * 16 + hg) * 512 + h * 128 + i * 8) = pk4(orem);
  ((unsigned long long*)(ws + (64u << 20)))[blockIdx.x * 8 + w] = tstart_;
  UNTIL(3, 25);
}

DI u32x4 gload16(const void* base, unsigned voff) { u32x4 r; asm volatile("global_load_dwordx4 %0, %1, %2" : "=v"(r) : "v"(voff), "s"(base) : "memory"); return r; }
DI u32x2 gload8(const void* base, unsigned voff) { u32x2 r; asm volatile("global_load_dwordx2 %0, %1, %2" : "=v"(r) : "v"(voff), "s"(base) : "memory"); return r; }
DI float gload4(const void* base, unsigned voff) { float r; asm volatile("global_load_dword %0, %1, %2" : "=v"(r) : "v"(voff), "s"(base) : "memory"); return r; }

DI void oproj_stage(f32x4 (&acc)[5], const bf16x8 (&o)[4], unsigned wa) {
#pragma unroll
  for (int f = 0; f < 4; ++f) {
    bf16x8 wf[5];
#pragma unroll
    for (int n = 0; n < 5; ++n) wf[n] = lds_trfrag(wa + f * 5120 + n * 32, 1280);
#pragma unroll
    for (int n = 0; n < 5; ++n) acc[n] = mfma(wf[n], o[f], acc[n]);
  }
}

__global__ __launch_bounds__(512, 2) void k_oproj(const char* __restrict__ woA, const char* __restrict__ woB, const char* __restrict__ ws,
                                                  char* __restrict__ out) {
  const unsigned long long tstart_ = ((const unsigned long long*)(ws + (64u << 20)))[blockIdx.x * 8];
  UNTIL(2, 30);
  const int tid = threadIdx.x, lane = tid & 63;
  const int w = __builtin_amdgcn_readfirstlane(tid >> 6);
  const int b = blockIdx.x, xcd = b & 7, idx = b >> 3;
  const int R = xcd * 4 + (idx & 3), c = idx >> 2;
  const int rt = R >> 1, br = w >> 2, j = w & 3;
  const int p = j & 1, t = 2 * (R & 1) + (j >> 1), T = 4 * p + t;
  const int g = lane >> 4, i = lane & 15, q = i >> 2, pp = i & 3;
  constexpr int ST = 40960;

  unsigned vo[5], vr[5];
#pragma unroll
  for (int n = 0; n < 5; ++n) {
    int u = (j * 5 + n) * 64 + lane;
    int rho = u / 10, cc = u - rho * 10;
    vo[n] = ((rho >> 4) * 20 + (rho & 15)) * 1280 + cc * 16;
    int head = 8 * (rho >> 5) + 4 * ((rho >> 3) & 1) + 2 * ((rho >> 4) & 1) + ((rho >> 2) & 1);
    vr[n] = (head * 20 + 16 + (rho & 3)) * 1280 + cc * 16;
  }
  const char* wo = (br ? woB : woA) + c * 160;
  const char* om = ws + (size_t)((rt * 8 + T) * 2 + br) * 16384;
  const char* orm = ws + OMAIN_B + ((size_t)(rt * 8 + T) * 16 + 8 * br) * 512;
  const unsigned ldsw = w * 5120;
  u32x4 of[16];
  u32x2 orl[4], orh[4];
  float gv = gload4(ws + OMAIN_B + OREM_B + (size_t)(rt * 8 + T) * 64, i * 4);
#pragma unroll
  for (int s = 0; s < 4; ++s) {
#pragma unroll
    for (int n = 0; n < 5; ++n) dma16(ldsw + s * ST + n * 1024, vo[n], wo + s * 160 * 1280);
#pragma unroll
    for (int f = 0; f < 4; ++f) of[s * 4 + f] = gload16(om + (s * 4 + f) * 1024, lane * 16);
  }
#pragma unroll
  for (int f = 0; f < 4; ++f) { orl[f] = gload8(orm + f * 1024, lane * 8); orh[f] = gload8(orm + f * 1024 + 512, lane * 8); }

  const unsigned wa = br * 20480 + (16 * (g >> 1) + 4 * (g & 1) + q) * 160 + 8 * pp;
  f32x4 acc[5] = {};
  bf16x8 o[4];
#define OWAIT(n, a, b2, c2, d) asm volatile("s_waitcnt vmcnt(" #n ") lgkmcnt(0)\n\ts_barrier" : "+v"(a), "+v"(b2), "+v"(c2), "+v"(d)::"memory")
  OWAIT(35, of[0], of[1], of[2], of[3]);
  asm volatile("" : "+v"(gv));
  STAMP(5);
#pragma unroll
  for (int f = 0; f < 4; ++f) o[f] = __builtin_bit_cast(bf16x8, of[f]);
  oproj_stage(acc, o, wa);
  OWAIT(26, of[4], of[5], of[6], of[7]);
#pragma unroll
  for (int n = 0; n < 5; ++n) dma16(ldsw + n * 1024, vr[n], wo);
#pragma unroll
  for (int f = 0; f < 4; ++f) o[f] = __builtin_bit_cast(bf16x8, of[4 + f]);
  oproj_stage(acc, o, wa + ST);
  OWAIT(22, of[8], of[9], of[10], of[11]);
#pragma unroll
  for (int f = 0; f < 4; ++f) o[f] = __builtin_bit_cast(bf16x8, of[8 + f]);
  oproj_stage(acc, o, wa + 2 * ST);
  OWAIT(13, of[12], of[13], of[14], of[15]);
#pragma unroll
  for (int f = 0; f < 4; ++f) o[f] = __builtin_bit_cast(bf16x8, of[12 + f]);
  oproj_stage(acc, o, wa + 3 * ST);
  asm volatile("s_waitcnt vmcnt(0) lgkmcnt(0)\n\ts_barrier"
               : "+v"(orl[0]), "+v"(orl[1]), "+v"(orl[2]), "+v"(orl[3]), "+v"(orh[0]), "+v"(orh[1]), "+v"(orh[2]), "+v"(orh[3])::"memory");
#pragma unroll
  for (int f = 0; f < 4; ++f) o[f] = cat(orl[f], orh[f]);
  oproj_stage(acc, o, wa);

  STAMP(6);
  const float sc = br ? 1.f - gv : gv;
#pragma unroll
  for (int n = 0; n < 5; ++n) acc[n] *= sc;
  const unsigned ex = ST + j * 5120 + lane * 16;
  char* orow = out + (size_t)(rt * 128 + 32 * t + 2 * i + p) * 1280 + c * 160;
  if (br) {
    *(LAS f32x4*)(ex) = acc[0]; *(LAS f32x4*)(ex + 1024) = acc[1]; *(LAS f32x4*)(ex + 4096) = acc[4];
  } else {
    *(LAS f32x4*)(ex + 2048) = acc[2]; *(LAS f32x4*)(ex + 3072) = acc[3];
  }
  asm volatile("s_waitcnt lgkmcnt(0)\n\ts_barrier" ::: "memory");
  const int n0 = br ? 2 : 0;
  f32x4 a0 = (br ? acc[2] : acc[0]) + *(const LAS f32x4*)(ex + n0 * 1024);
  f32x4 a1 = (br ? acc[3] : acc[1]) + *(const LAS f32x4*)(ex + n0 * 1024 + 1024);
  *(u32x4*)(orow + n0 * 32 + (g & 1) * 32 + (g >> 1) * 16) = swap16x2(pk4(a0), pk4(a1));
  if (!br) {
    f32x4 a4 = acc[4] + *(const LAS f32x4*)(ex + 4096);
    *(u32x2*)(orow + 128 + g * 8) = pk4(a4);
  }
  UNTIL(1, 50);
}

extern "C" void kernel_launch(void* const* d_in, const int* in_sizes, int n_in, void* d_out, int out_size, void* d_ws, size_t ws_size,
                              hipStream_t stream) {
  (void)hipFuncSetAttribute((const void*)k_qkv_attn, hipFuncAttributeMaxDynamicSharedMemorySize, LDS1);
  (void)hipFuncSetAttribute((const void*)k_oproj, hipFuncAttributeMaxDynamicSharedMemorySize, LDS2);
  k_qkv_attn<<<256, 512, LDS1, stream>>>((const char*)d_in[0], (const char*)d_in[1], (const char*)d_in[4], (const char*)d_in[6], (char*)d_ws);
  k_oproj<<<256, 512, LDS2, stream>>>((const char*)d_in[2], (const char*)d_in[5], (const char*)d_ws, (char*)d_out);
}
```

```cpp
#define EXPT 3
#include <hip/hip_runtime.h>
#include <stdint.h>

typedef __bf16 bf16x8 __attribute__((ext_vector_type(8)));
typedef __bf16 bf16x2 __attribute__((ext_vector_type(2)));
typedef short s16x4 __attribute__((ext_vector_type(4)));
typedef float f32x4 __attribute__((ext_vector_type(4)));
typedef float f32x2 __attribute__((ext_vector_type(2)));
typedef unsigned u32x2 __attribute__((ext_vector_type(2)));
typedef unsigned u32x4 __attribute__((ext_vector_type(4)));
#define LAS __attribute__((address_space(3)))
#define DI __device__ __forceinline__

constexpr int SLOT = 49152;
constexpr int XB = 18432;
constexpr int WKS = 15360;
constexpr int LDS_GATE = 3 * SLOT;
constexpr int LDS_ZERO = LDS_GATE + 2048;
constexpr int LDS1 = LDS_ZERO + 64;
constexpr int EXCH = SLOT;
constexpr int LDS2 = 163840;

constexpr size_t OMAIN_B = 4u << 20, OREM_B = 1u << 20;

DI bf16x8 lds_b128(unsigned a) { return __builtin_bit_cast(bf16x8, *(const LAS u32x4*)a); }
DI u32x2 lds_b64(unsigned a) { return *(const LAS u32x2*)a; }
DI u32x2 lds_tr(unsigned a) { return __builtin_bit_cast(u32x2, __builtin_amdgcn_ds_read_tr16_b64_v4i16((LAS s16x4*)a)); }
DI bf16x8 cat(u32x2 a, u32x2 b) { u32x4 r = {a.x, a.y, b.x, b.y}; return __builtin_bit_cast(bf16x8, r); }
DI bf16x8 lds_trfrag(unsigned a, int ustride) { return cat(lds_tr(a), lds_tr(a + ustride)); }
DI f32x4 mfma(bf16x8 a, bf16x8 b, f32x4 c) { return __builtin_amdgcn_mfma_f32_16x16x32_bf16(a, b, c, 0, 0, 0); }
DI unsigned pk(float a, float b) { f32x2 v = {a, b}; return __builtin_bit_cast(unsigned, __builtin_convertvector(v, bf16x2)); }
DI u32x2 pk4(f32x4 v) { u32x2 r = {pk(v[0], v[1]), pk(v[2], v[3])}; return r; }
DI void dma16(unsigned lds, unsigned voff, const void* base) {
  asm volatile("s_mov_b32 m0, %0\n\ts_nop 0\n\tglobal_load_lds_dwordx4 %1, %2" ::"s"(lds), "v"(voff), "s"(base) : "memory");
}
#define WAIT_BAR(n) asm volatile("s_waitcnt vmcnt(" #n ") lgkmcnt(0)\n\ts_barrier" ::: "memory")
DI void swap32(unsigned& a, unsigned& b) { auto r = __builtin_amdgcn_permlane32_swap(a, b, false, false); a = r[0]; b = r[1]; }
DI void swap16(unsigned& a, unsigned& b) { auto r = __builtin_amdgcn_permlane16_swap(a, b, false, false); a = r[0]; b = r[1]; }
DI u32x4 swap32x2(u32x2 a, u32x2 b) { unsigned ax = a.x, ay = a.y, bx = b.x, by = b.y; swap32(ax, bx); swap32(ay, by); u32x4 r = {ax, ay, bx, by}; return r; }
DI u32x4 swap16x2(u32x2 a, u32x2 b) { unsigned ax = a.x, ay = a.y, bx = b.x, by = b.y; swap16(ax, bx); swap16(ay, by); u32x4 r = {ax, ay, bx, by}; return r; }
DI void fswap32(float& a, float& b) { unsigned x = __float_as_uint(a), y = __float_as_uint(b); swap32(x, y); a = __uint_as_float(x); b = __uint_as_float(y); }
DI void fswap16(float& a, float& b) { unsigned x = __float_as_uint(a), y = __float_as_uint(b); swap16(x, y); a = __uint_as_float(x); b = __uint_as_float(y); }

#define STAMP(k)
#define STAMP_DECL
#define STAMP_FLUSH(a, b2)
template <int ROLE>
struct Frags {
  bf16x8 x[4], w[3], e[ROLE == 2 ? 4 : (ROLE == 1 ? 3 : 1)];
};

template <int ROLE>
DI void load_frags(Frags<ROLE>& f, unsigned xa, unsigned wm, unsigned wr, unsigned wa, unsigned xg, unsigned ga) {
#pragma unroll
  for (int t = 0; t < 4; ++t) f.x[t] = lds_b128(xa + t * 2048);
#pragma unroll
  for (int s = 0; s < 3; ++s) f.w[s] = lds_trfrag(wm + s * 160, 3840);
  f.e[0] = lds_trfrag(wr, 3840);
  if constexpr (ROLE == 1) { f.e[1] = lds_b128(xg); f.e[2] = lds_b128(ga); }
  if constexpr (ROLE == 2) { f.e[1] = lds_b128(xg); f.e[2] = lds_trfrag(wa + 160, 3840); f.e[3] = lds_trfrag(wa + 320, 3840); }
}
template <int ROLE>
DI void mma_step(const Frags<ROLE>& f, f32x4 (&aq)[4], f32x4 (&ak)[4], f32x4 (&av)[4], f32x4 (&ae)[5]) {
#pragma unroll
  for (int t = 0; t < 4; ++t) {
    aq[t] = mfma(f.w[0], f.x[t], aq[t]);
    ak[t] = mfma(f.w[1], f.x[t], ak[t]);
    av[t] = mfma(f.x[t], f.w[2], av[t]);
    if constexpr (ROLE != 2) ae[t] = mfma(f.e[0], f.x[t], ae[t]);
  }
  if constexpr (ROLE == 1) ae[4] = mfma(f.e[2], f.e[1], ae[4]);
  if constexpr (ROLE == 2) {
    ae[0] = mfma(f.w[1], f.e[1], ae[0]);
    ae[1] = mfma(f.w[2], f.e[1], ae[1]);
    ae[2] = mfma(f.e[2], f.e[1], ae[2]);
    ae[3] = mfma(f.e[3], f.e[1], ae[3]);
    ae[4] = mfma(f.e[0], f.e[1], ae[4]);
  }
}

template <int ROLE>
DI void kloop(f32x4 (&aq)[4], f32x4 (&ak)[4], f32x4 (&av)[4], f32x4 (&ae)[5], unsigned xa, unsigned wm, unsigned wr, unsigned wa,
              unsigned xg, unsigned ga, const unsigned (&voff)[6], const char* gbase, unsigned gstep, unsigned ldsw) {
  Frags<ROLE> f0, f1;
  WAIT_BAR(12);
  load_frags<ROLE>(f0, xa, wm, wr, wa, xg, ga);
  unsigned so = 0;
  for (int s = 0; s < 10; ++s) {
    load_frags<ROLE>(f1, (xa ^ 64) + so, wm + so + WKS, wr + so + WKS, wa + so + WKS, (xg ^ 64) + so, ga + 64);
    mma_step<ROLE>(f0, aq, ak, av, ae);
    __builtin_amdgcn_sched_barrier(0);
    if (s < 9) {
      if (s < 8) WAIT_BAR(6); else WAIT_BAR(0);
      if (s < 7) {
#pragma unroll
        for (int j = 0; j < 6; ++j) dma16(ldsw + so + j * 1024, voff[j], gbase);
        gbase += gstep;
      }
      so = (so == 2 * SLOT) ? 0 : so + SLOT;
      ga += 128;
      load_frags<ROLE>(f0, xa + so, wm + so, wr + so, wa + so, xg + so, ga);
    }
    mma_step<ROLE>(f1, aq, ak, av, ae);
    __builtin_amdgcn_sched_barrier(0);
  }
}

__global__ __launch_bounds__(512, 2) void k_qkv_attn(const char* __restrict__ x, const char* __restrict__ wA, const char* __restrict__ wB,
                                                     const char* __restrict__ gatew, char* __restrict__ ws) {
  const unsigned long long tstart_ = __builtin_amdgcn_s_memrealtime();
  const int tid = threadIdx.x, lane = tid & 63;
  const int w = __builtin_amdgcn_readfirstlane(tid >> 6);
  const int b = blockIdx.x, xcd = b & 7, idx = b >> 3;
  const int rt = (xcd >> 2) * 8 + (idx & 7), hg = (xcd & 3) * 4 + (idx >> 3);
  const int br = hg >> 3, hl = hg & 7;
  const int g = lane >> 4, i = lane & 15, q = i >> 2, pp = i & 3;
  const int p = w >> 2, h = w & 3;

  unsigned voff[6];
  const char* gbase;
  unsigned gstep;
  if (w < 3) {
    gbase = x; gstep = 128;
#pragma unroll
    for (int j = 0; j < 6; ++j) {
      int rho = (w * 6 + j) * 8 + (lane >> 3);
      int T = rho >> 4, ii = rho & 15;
      int row = (T < 8) ? rt * 128 + (T & 3) * 32 + 2 * ii + (T >> 2) : rt * 128 - 16 + 2 * (ii & 7) + (ii < 8 ? 1 : 0);
      row = row < 0 ? 0 : row;
      voff[j] = row * 1280 + (((lane & 7) ^ (ii >> 1)) << 4);
    }
  } else {
    gbase = (br ? wB : wA) + hl * 160; gstep = 64 * 3840;
#pragma unroll
    for (int j = 0; j < 6; ++j) {
      int u = ((w - 3) * 6 + j) * 64 + lane;
      int rho = u / 30, c = u - rho * 30;
      int kap = (rho & ~12) | ((rho & 4) << 1) | ((rho & 8) >> 1);
      int sct = c / 10;
      voff[j] = kap * 3840 + sct * 1280 + (c - sct * 10) * 16;
    }
  }
  const unsigned ldsw = w * 6144;
  if (w == 0) {
    dma16(LDS_GATE, lane * 16, gatew);
    dma16(LDS_GATE + 1024, 1024 + (lane & 15) * 16, gatew);
  }
#pragma unroll
  for (int s = 0; s < 3; ++s) {
#pragma unroll
    for (int j = 0; j < 6; ++j) dma16(ldsw + s * SLOT + j * 1024, voff[j], gbase);
    gbase += gstep;
  }
  STAMP(1);
  if (tid < 16) *(LAS unsigned*)(LDS_ZERO + tid * 4) = 0u;

  const int sec = (0x12201102 >> (4 * w)) & 3;
  const unsigned xl = i * 128 + ((g ^ (i >> 1)) << 4);
  const unsigned xa = p * 8192 + xl;
  const unsigned wrow = XB + (16 * (g >> 1) + 4 * (g & 1) + q) * 480;
  const unsigned wm = wrow + 40 * h + 8 * pp;
  const unsigned wr = wrow + sec * 160 + 40 * pp + 32;
  const unsigned wa = wrow + 40 * p + 8 * pp;
  const bool halo = (w == 3 || w == 6), gate = (w == 0 || w == 5);
  const int Tg = 2 * (hg & 3) + p;
  const unsigned xg = (halo ? 8 : Tg) * 2048 + xl;
  const unsigned ga = LDS_GATE + g * 16;

  f32x4 aq[4] = {}, ak[4] = {}, av[4] = {}, ae[5] = {};
  if (halo) kloop<2>(aq, ak, av, ae, xa, wm, wr, wa, xg, ga, voff, gbase, gstep, ldsw);
  else if (gate) kloop<1>(aq, ak, av, ae, xa, wm, wr, wa, xg, ga, voff, gbase, gstep, ldsw);
  else kloop<0>(aq, ak, av, ae, xa, wm, wr, wa, xg, ga, voff, gbase, gstep, ldsw);

  STAMP(2);
  const unsigned lin = EXCH + lane * 8, trw = EXCH + i * 32 + ((g ^ q) << 3);
  if (halo) {
    *(LAS u32x2*)(lin + (24 + h) * 512) = pk4(ae[0]);
    *(LAS u32x2*)(trw + (28 + h) * 512) = pk4(ae[1]);
    *(LAS u32x2*)(lin + (24 + p) * 512) = pk4(ae[2]);
    *(LAS u32x2*)(trw + (28 + p) * 512) = pk4(ae[3]);
    *(LAS u32x2*)((p ? trw : lin) + (32 + p) * 512) = pk4(ae[4]);
  } else {
    const unsigned wb = (sec == 2 ? trw : lin) + (sec * 8 + p * 4) * 512;
#pragma unroll
    for (int t = 0; t < 4; ++t) *(LAS u32x2*)(wb + t * 512) = pk4(ae[t]);
    if (gate && hg < 4 && lane < 16) {
      float gv = 1.f / (1.f + __expf(-ae[4][0]));
      *(float*)(ws + OMAIN_B + OREM_B + (size_t)(rt * 128 + Tg * 16 + i) * 4) = gv;
    }
  }
  u32x2 Q[4], K[5], V[5];
#pragma unroll
  for (int t = 0; t < 4; ++t) { Q[t] = pk4(aq[t]); K[t + 1] = pk4(ak[t]); V[t + 1] = pk4(av[t]); }
  asm volatile("s_waitcnt lgkmcnt(0)\n\ts_barrier" ::: "memory");
  u32x2 Qr[4], Kr[5], Vp[4], Vc[4];
  const unsigned trr = EXCH + (4 * g + q) * 32;
  K[0] = lds_b64(lin + (24 + h) * 512);
  V[0] = lds_tr(trr + ((pp ^ g) << 3) + (28 + h) * 512);
  Kr[0] = lds_b64(lin + 32 * 512);
  const unsigned vr = trr + ((h ^ g) << 3);
#pragma unroll
  for (int t = 0; t < 4; ++t) {
    Qr[t] = lds_b64(g == h ? lin + (p * 4 + t) * 512 : LDS_ZERO);
    Kr[t + 1] = lds_b64(lin + (8 + p * 4 + t) * 512);
    Vp[t] = lds_tr(pp == t ? vr + (t == 0 ? 33 : 16 + p * 4 + t - 1) * 512 : LDS_ZERO);
    Vc[t] = lds_tr(pp == t ? vr + (16 + p * 4 + t) * 512 : LDS_ZERO);
  }
  const float NEG = -1e30f;
  f32x4 bc, bp, b0;
#pragma unroll
  for (int r = 0; r < 4; ++r) {
    int j = 4 * g + r;
    bc[r] = (j <= i && j >= i - 7) ? 0.f : NEG;
    bp[r] = (j >= i + 9) ? 0.f : NEG;
    b0[r] = (rt > 0 && (p ? (j > i && j < 8) : (j >= i + 9))) ? 0.f : NEG;
  }
  const unsigned mc0 = (4 * g <= i ? 0xffffu : 0u) | (4 * g + 1 <= i ? 0xffff0000u : 0u);
  const unsigned mc1 = (4 * g + 2 <= i ? 0xffffu : 0u) | (4 * g + 3 <= i ? 0xffff0000u : 0u);
  f32x4 sc[4];
  float mx[4];
#pragma unroll
  for (int t = 0; t < 4; ++t) {
    bf16x8 qo = cat(Q[t], Qr[t]);
    f32x4 s1 = mfma(cat(K[t + 1], Kr[t + 1]), qo, bc);
    f32x4 s0 = mfma(cat(K[t], Kr[t]), qo, t == 0 ? b0 : bp);
#pragma unroll
    for (int r = 0; r < 4; ++r) sc[t][r] = fmaxf(s0[r], s1[r]);
    mx[t] = fmaxf(fmaxf(sc[t][0], sc[t][1]), fmaxf(sc[t][2], sc[t][3]));
  }
  const float cs = 0.22360679774997896f * 1.4426950408889634f;
  {
    fswap32(mx[0], mx[1]); fswap32(mx[2], mx[3]);
    float a = fmaxf(mx[0], mx[1]), c = fmaxf(mx[2], mx[3]);
    fswap16(a, c);
    float m = fmaxf(a, c) * cs, m2 = m;
    fswap16(m, m2);
    mx[0] = m; mx[1] = m; fswap32(mx[0], mx[1]);
    mx[2] = m2; mx[3] = m2; fswap32(mx[2], mx[3]);
  }
  float sm[4];
#pragma unroll
  for (int t = 0; t < 4; ++t) {
#pragma unroll
    for (int r = 0; r < 4; ++r) sc[t][r] = __builtin_amdgcn_exp2f(__builtin_fmaf(sc[t][r], cs, -mx[t]));
    sm[t] = (sc[t][0] + sc[t][1]) + (sc[t][2] + sc[t][3]);
  }
  {
    fswap32(sm[0], sm[1]); fswap32(sm[2], sm[3]);
    float a = sm[0] + sm[1], c = sm[2] + sm[3];
    fswap16(a, c);
    float m = __builtin_amdgcn_rcpf(a + c), m2 = m;
    fswap16(m, m2);
    sm[0] = m; sm[1] = m; fswap32(sm[0], sm[1]);
    sm[2] = m2; sm[3] = m2; fswap32(sm[2], sm[3]);
  }
  f32x4 orem = {};
  u32x2 O[4];
#pragma unroll
  for (int t = 0; t < 4; ++t) {
    u32x2 pb = pk4(sc[t] * sm[t]);
    u32x2 pc = {pb.x & mc0, pb.y & mc1}, pv = {pb.x & ~mc0, pb.y & ~mc1};
    bf16x8 po = cat(pv, pc);
    f32x4 z = {};
    O[t] = pk4(mfma(cat(V[t], V[t + 1]), po, z));
    orem = mfma(cat(Vp[t], Vc[t]), po, orem);
  }
  char* om = ws + ((size_t)((rt * 8 + p * 4 + (g >> 1)) * 2 + br) * 16 + 2 * hl + (h >> 1)) * 1024 + ((h & 1) * 32 + (lane & 31)) * 16;
#pragma unroll
  for (int m = 0; m < 2; ++m) {
    *(u32x4*)(om + (size_t)m * 2 * 32768) = swap32x2(O[2 * m], O[2 * m + 1]);
  }
  *(u32x2*)(ws + OMAIN_B + ((size_t)(rt * 8 + p * 4 + g) * 16 + hg) * 512 + h * 128 + i * 8) = pk4(orem);
  if (!(EXPT & 1)) ((unsigned long long*)(ws + (64u << 20)))[blockIdx.x * 8 + w] = tstart_;
}

DI u32x4 gload16(const void* base, unsigned voff) { u32x4 r; asm volatile("global_load_dwordx4 %0, %1, %2" : "=v"(r) : "v"(voff), "s"(base) : "memory"); return r; }
DI u32x2 gload8(const void* base, unsigned voff) { u32x2 r; asm volatile("global_load_dwordx2 %0, %1, %2" : "=v"(r) : "v"(voff), "s"(base) : "memory"); return r; }
DI float gload4(const void* base, unsigned voff) { float r; asm volatile("global_load_dword %0, %1, %2" : "=v"(r) : "v"(voff), "s"(base) : "memory"); return r; }

DI void oproj_stage(f32x4 (&acc)[5], const bf16x8 (&o)[4], unsigned wa) {
#pragma unroll
  for (int f = 0; f < 4; ++f) {
    bf16x8 wf[5];
#pragma unroll
    for (int n = 0; n < 5; ++n) wf[n] = lds_trfrag(wa + f * 5120 + n * 32, 1280);
#pragma unroll
    for (int n = 0; n < 5; ++n) acc[n] = mfma(wf[n], o[f], acc[n]);
  }
}

__global__ __launch_bounds__(512, 2) void k_oproj(const char* __restrict__ woA, const char* __restrict__ woB, const char* __restrict__ ws,
                                                  char* __restrict__ out) {
  const unsigned long long tstart_ = ((const unsigned long long*)(ws + (64u << 20)))[0];
  const int tid = threadIdx.x, lane = tid & 63;
  const int w = __builtin_amdgcn_readfirstlane(tid >> 6);
  const int b = blockIdx.x, xcd = b & 7, idx = b >> 3;
  const int R = xcd * 4 + (idx & 3), c = idx >> 2;
  const int rt = R >> 1, br = w >> 2, j = w & 3;
  const int p = j & 1, t = 2 * (R & 1) + (j >> 1), T = 4 * p + t;
  const int g = lane >> 4, i = lane & 15, q = i >> 2, pp = i & 3;
  constexpr int ST = 40960;

  unsigned vo[5], vr[5];
#pragma unroll
  for (int n = 0; n < 5; ++n) {
    int u = (j * 5 + n) * 64 + lane;
    int rho = u / 10, cc = u - rho * 10;
    vo[n] = ((rho >> 4) * 20 + (rho & 15)) * 1280 + cc * 16;
    int head = 8 * (rho >> 5) + 4 * ((rho >> 3) & 1) + 2 * ((rho >> 4) & 1) + ((rho >> 2) & 1);
    vr[n] = (head * 20 + 16 + (rho & 3)) * 1280 + cc * 16;
  }
  const char* wo = (br ? woB : woA) + c * 160;
  const char* om = ws + (size_t)((rt * 8 + T) * 2 + br) * 16384;
  const char* orm = ws + OMAIN_B + ((size_t)(rt * 8 + T) * 16 + 8 * br) * 512;
  const unsigned ldsw = w * 5120;
  u32x4 of[16];
  u32x2 orl[4], orh[4];
  float gv = gload4(ws + OMAIN_B + OREM_B + (size_t)(rt * 8 + T) * 64, i * 4);
#pragma unroll
  for (int s = 0; s < 4; ++s) {
#pragma unroll
    for (int n = 0; n < 5; ++n) dma16(ldsw + s * ST + n * 1024, vo[n], wo + s * 160 * 1280);
#pragma unroll
    for (int f = 0; f < 4; ++f) of[s * 4 + f] = gload16(om + (s * 4 + f) * 1024, lane * 16);
  }
#pragma unroll
  for (int f = 0; f < 4; ++f) { orl[f] = gload8(orm + f * 1024, lane * 8); orh[f] = gload8(orm + f * 1024 + 512, lane * 8); }

  const unsigned wa = br * 20480 + (16 * (g >> 1) + 4 * (g & 1) + q) * 160 + 8 * pp;
  f32x4 acc[5] = {};
  bf16x8 o[4];
#define OWAIT(n, a, b2, c2, d) asm volatile("s_waitcnt vmcnt(" #n ") lgkmcnt(0)\n\ts_barrier" : "+v"(a), "+v"(b2), "+v"(c2), "+v"(d)::"memory")
  OWAIT(35, of[0], of[1], of[2], of[3]);
  asm volatile("" : "+v"(gv));
  STAMP(5);
#pragma unroll
  for (int f = 0; f < 4; ++f) o[f] = __builtin_bit_cast(bf16x8, of[f]);
  oproj_stage(acc, o, wa);
  OWAIT(26, of[4], of[5], of[6], of[7]);
#pragma unroll
  for (int n = 0; n < 5; ++n) dma16(ldsw + n * 1024, vr[n], wo);
#pragma unroll
  for (int f = 0; f < 4; ++f) o[f] = __builtin_bit_cast(bf16x8, of[4 + f]);
  oproj_stage(acc, o, wa + ST);
  OWAIT(22, of[8], of[9], of[10], of[11]);
#pragma unroll
  for (int f = 0; f < 4; ++f) o[f] = __builtin_bit_cast(bf16x8, of[8 + f]);
  oproj_stage(acc, o, wa + 2 * ST);
  OWAIT(13, of[12], of[13], of[14], of[15]);
#pragma unroll
  for (int f = 0; f < 4; ++f) o[f] = __builtin_bit_cast(bf16x8, of[12 + f]);
  oproj_stage(acc, o, wa + 3 * ST);
  asm volatile("s_waitcnt vmcnt(0) lgkmcnt(0)\n\ts_barrier"
               : "+v"(orl[0]), "+v"(orl[1]), "+v"(orl[2]), "+v"(orl[3]), "+v"(orh[0]), "+v"(orh[1]), "+v"(orh[2]), "+v"(orh[3])::"memory");
#pragma unroll
  for (int f = 0; f < 4; ++f) o[f] = cat(orl[f], orh[f]);
  oproj_stage(acc, o, wa);

  STAMP(6);
  const float sc = br ? 1.f - gv : gv;
#pragma unroll
  for (int n = 0; n < 5; ++n) acc[n] *= sc;
  const unsigned ex = ST + j * 5120 + lane * 16;
  char* orow = out + (size_t)(rt * 128 + 32 * t + 2 * i + p) * 1280 + c * 160;
  if (br) {
    *(LAS f32x4*)(ex) = acc[0]; *(LAS f32x4*)(ex + 1024) = acc[1]; *(LAS f32x4*)(ex + 4096) = acc[4];
  } else {
    *(LAS f32x4*)(ex + 2048) = acc[2]; *(LAS f32x4*)(ex + 3072) = acc[3];
  }
  asm volatile("s_waitcnt lgkmcnt(0)\n\ts_barrier" ::: "memory");
  const int n0 = br ? 2 : 0;
  f32x4 a0 = (br ? acc[2] : acc[0]) + *(const LAS f32x4*)(ex + n0 * 1024);
  f32x4 a1 = (br ? acc[3] : acc[1]) + *(const LAS f32x4*)(ex + n0 * 1024 + 1024);
  *(u32x4*)(orow + n0 * 32 + (g & 1) * 32 + (g >> 1) * 16) = swap16x2(pk4(a0), pk4(a1));
  if (!br) {
    f32x4 a4 = acc[4] + *(const LAS f32x4*)(ex + 4096);
    *(u32x2*)(orow + 128 + g * 8) = pk4(a4);
  }
  if (!(EXPT & 2)) { while (__builtin_amdgcn_s_memrealtime() - tstart_ < 5000) __builtin_amdgcn_s_sleep(1); }
}

__global__ void k_first(char* ws) { ((unsigned long long*)(ws + (64u << 20)))[0] = __builtin_amdgcn_s_memrealtime(); }
__global__ void k_last(const char* ws) { const unsigned long long tstart_ = ((const unsigned long long*)(ws + (64u << 20)))[0];
  while (__builtin_amdgcn_s_memrealtime() - tstart_ < 5000) __builtin_amdgcn_s_sleep(1); }
extern "C" void kernel_launch(void* const* d_in, const int* in_sizes, int n_in, void* d_out, int out_size, void* d_ws, size_t ws_size,
                              hipStream_t stream) {
  (void)hipFuncSetAttribute((const void*)k_qkv_attn, hipFuncAttributeMaxDynamicSharedMemorySize, LDS1);
  (void)hipFuncSetAttribute((const void*)k_oproj, hipFuncAttributeMaxDynamicSharedMemorySize, LDS2);
  if (EXPT & 1) k_first<<<1, 64, 0, stream>>>((char*)d_ws);
  k_qkv_attn<<<256, 512, LDS1, stream>>>((const char*)d_in[0], (const char*)d_in[1], (const char*)d_in[4], (const char*)d_in[6], (char*)d_ws);
  k_oproj<<<256, 512, LDS2, stream>>>((const char*)d_in[2], (const char*)d_in[5], (const char*)d_ws, (char*)d_out);
  if (EXPT & 2) k_last<<<1, 64, 0, stream>>>((const char*)d_ws);
}
```

```cpp
#include <hip/hip_runtime.h>

__global__ __launch_bounds__(512, 2) void k_fused(const void* x, const void* wA, const void* wB, const void* gate, const void* woA,
                                                  const void* woB, void* ws, void* out) {
  asm volatile(
  "s_load_dwordx16 s[8:23], %0, 0\n"
  "s_mov_b32 s25, %1\n"
  "v_and_b32 v1, 63, %2\n"
  "v_lshrrev_b32 v2, 6, %2\n"
  "s_lshr_b32 s27, s25, 3\n"
  "s_and_b32 s26, s25, 7\n"
  "v_readfirstlane_b32 s24, v2\n"
  "s_getpc_b64 s[56:57]\n"
  ".Lpc:\n"
  "s_lshl_b32 s32, s24, 5\n"
  "s_add_u32 s32, s32, s27\n"
  "s_lshl_b32 s32, s32, 7\n"
  "s_min_u32 s32, s32, .Lend-.Lpc\n"
  "v_mov_b32 v2, s32\n"
  "s_mov_b32 m0, 163072\n"
  "s_nop 0\n"
  "global_load_lds_dword v2, s[56:57]\n"
  "s_lshr_b32 s32, s26, 2\n"
  "s_lshl_b32 s6, s32, 3\n"
  "s_and_b32 s33, s27, 7\n"
  "s_or_b32 s28, s6, s33\n"
  "s_and_b32 s7, s26, 3\n"
  "s_lshl_b32 s6, s7, 2\n"
  "s_lshr_b32 s34, s27, 3\n"
  "s_or_b32 s29, s6, s34\n"
  "s_mul_i32 s34, s34, 3\n"
  "s_add_u32 s37, s33, s34\n"
  "s_mul_i32 s7, s7, 3\n"
  "s_add_u32 s37, s37, s7\n"
  "s_mul_i32 s32, s32, 2\n"
  "s_add_u32 s37, s37, s32\n"
  "s_mul_i32 s32, s37, 205\n"
  "s_lshr_b32 s32, s32, 11\n"
  "s_mul_i32 s32, s32, 10\n"
  "s_sub_u32 s37, s37, s32\n"
  "s_mov_b32 s38, s37\n"
  "s_mul_i32 s39, s24, 6144\n"
  "s_mov_b32 s40, s39\n"
  "s_cmp_lt_u32 s24, 3\n"
  "s_cbranch_scc1 .Lx\n"
  "s_mov_b32 s35, 245760\n"
  "s_mov_b32 s36, -2211840\n"
  "s_sub_u32 s32, s24, 3\n"
  "s_mul_i32 s32, s32, 384\n"
  "v_add_u32 v5, s32, v1\n"
  "s_movk_i32 s49, -30\n"
  "s_movk_i32 s41, 1120\n"
  "v_mul_u32_u24 v2, 2185, v5\n"
  "v_lshrrev_b32 v2, 16, v2\n"
  "v_mad_i32_i24 v3, v2, s49, v5\n"
  "v_lshrrev_b32 v4, 1, v2\n"
  "v_bitop3_b32 v4, v4, v2, 4 bitop3:0x28\n"
  "v_mul_u32_u24 v4, 3, v4\n"
  "v_xor_b32 v2, v2, v4\n"
  "v_mul_u32_u24 v2, 3840, v2\n"
  "v_lshl_add_u32 v2, v3, 4, v2\n"
  "v_mul_u32_u24 v3, 26, v3\n"
  "v_lshrrev_b32 v3, 8, v3\n"
  "s_and_b32 s32, s29, 7\n"
  "s_mul_i32 s32, s32, 160\n"
  "s_mul_i32 s33, s37, 245760\n"
  "s_add_u32 s32, s32, s33\n"
  "s_cmp_ge_u32 s29, 8\n"
  "s_waitcnt lgkmcnt(0)\n"
  "s_cselect_b32 s52, s12, s10\n"
  "s_cselect_b32 s53, s13, s11\n"
  "s_add_u32 s52, s52, s32\n"
  "s_addc_u32 s53, s53, 0\n"
  "s_add_u32 m0, s40, 0\n"
  "v_mad_u32_u24 v8, v3, s41, v2\n"
  "global_load_lds_dwordx4 v8, s[52:53]\n"
  "v_add_u32 v5, 64, v5\n"
  "v_mul_u32_u24 v2, 2185, v5\n"
  "v_lshrrev_b32 v2, 16, v2\n"
  "v_mad_i32_i24 v3, v2, s49, v5\n"
  "v_lshrrev_b32 v4, 1, v2\n"
  "v_bitop3_b32 v4, v4, v2, 4 bitop3:0x28\n"
  "v_mul_u32_u24 v4, 3, v4\n"
  "v_xor_b32 v2, v2, v4\n"
  "v_mul_u32_u24 v2, 3840, v2\n"
  "v_lshl_add_u32 v2, v3, 4, v2\n"
  "v_mul_u32_u24 v3, 26, v3\n"
  "v_lshrrev_b32 v3, 8, v3\n"
  "s_add_u32 m0, s40, 1024\n"
  "v_mad_u32_u24 v9, v3, s41, v2\n"
  "global_load_lds_dwordx4 v9, s[52:53]\n"
  "v_add_u32 v5, 64, v5\n"
  "v_mul_u32_u24 v2, 2185, v5\n"
  "v_lshrrev_b32 v2, 16, v2\n"
  "v_mad_i32_i24 v3, v2, s49, v5\n"
  "v_lshrrev_b32 v4, 1, v2\n"
  "v_bitop3_b32 v4, v4, v2, 4 bitop3:0x28\n"
  "v_mul_u32_u24 v4, 3, v4\n"
  "v_xor_b32 v2, v2, v4\n"
  "v_mul_u32_u24 v2, 3840, v2\n"
  "v_lshl_add_u32 v2, v3, 4, v2\n"
  "v_mul_u32_u24 v3, 26, v3\n"
  "v_lshrrev_b32 v3, 8, v3\n"
  "s_add_u32 m0, s40, 2048\n"
  "v_mad_u32_u24 v10, v3, s41, v2\n"
  "global_load_lds_dwordx4 v10, s[52:53]\n"
  "v_add_u32 v5, 64, v5\n"
  "v_mul_u32_u24 v2, 2185, v5\n"
  "v_lshrrev_b32 v2, 16, v2\n"
  "v_mad_i32_i24 v3, v2, s49, v5\n"
  "v_lshrrev_b32 v4, 1, v2\n"
  "v_bitop3_b32 v4, v4, v2, 4 bitop3:0x28\n"
  "v_mul_u32_u24 v4, 3, v4\n"
  "v_xor_b32 v2, v2, v4\n"
  "v_mul_u32_u24 v2, 3840, v2\n"
  "v_lshl_add_u32 v2, v3, 4, v2\n"
  "v_mul_u32_u24 v3, 26, v3\n"
  "v_lshrrev_b32 v3, 8, v3\n"
  "s_add_u32 m0, s40, 3072\n"
  "v_mad_u32_u24 v11, v3, s41, v2\n"
  "global_load_lds_dwordx4 v11, s[52:53]\n"
  "v_add_u32 v5, 64, v5\n"
  "v_mul_u32_u24 v2, 2185, v5\n"
  "v_lshrrev_b32 v2, 16, v2\n"
  "v_mad_i32_i24 v3, v2, s49, v5\n"
  "v_lshrrev_b32 v4, 1, v2\n"
  "v_bitop3_b32 v4, v4, v2, 4 bitop3:0x28\n"
  "v_mul_u32_u24 v4, 3, v4\n"
  "v_xor_b32 v2, v2, v4\n"
  "v_mul_u32_u24 v2, 3840, v2\n"
  "v_lshl_add_u32 v2, v3, 4, v2\n"
  "v_mul_u32_u24 v3, 26, v3\n"
  "v_lshrrev_b32 v3, 8, v3\n"
  "s_add_u32 m0, s40, 4096\n"
  "v_mad_u32_u24 v12, v3, s41, v2\n"
  "global_load_lds_dwordx4 v12, s[52:53]\n"
  "v_add_u32 v5, 64, v5\n"
  "v_mul_u32_u24 v2, 2185, v5\n"
  "v_lshrrev_b32 v2, 16, v2\n"
  "v_mad_i32_i24 v3, v2, s49, v5\n"
  "v_lshrrev_b32 v4, 1, v2\n"
  "v_bitop3_b32 v4, v4, v2, 4 bitop3:0x28\n"
  "v_mul_u32_u24 v4, 3, v4\n"
  "v_xor_b32 v2, v2, v4\n"
  "v_mul_u32_u24 v2, 3840, v2\n"
  "v_lshl_add_u32 v2, v3, 4, v2\n"
  "v_mul_u32_u24 v3, 26, v3\n"
  "v_lshrrev_b32 v3, 8, v3\n"
  "s_add_u32 m0, s40, 5120\n"
  "v_mad_u32_u24 v13, v3, s41, v2\n"
  "global_load_lds_dwordx4 v13, s[52:53]\n"
  "v_lshlrev_b32 v66, 4, v1\n"
  "s_branch .Lcom\n"
  ".Lx:\n"
  "v_lshrrev_b32 v2, 3, v1\n"
  "v_mul_u32_u24 v2, 2560, v2\n"
  "v_lshrrev_b32 v3, 4, v1\n"
  "v_xor_b32 v3, v3, v1\n"
  "v_and_b32 v3, 7, v3\n"
  "v_lshl_add_u32 v2, v3, 4, v2\n"
  "v_xor_b32 v3, 64, v2\n"
  "s_movk_i32 s35, 128\n"
  "s_movk_i32 s36, -1152\n"
  "s_mul_i32 s32, s24, 3\n"
  "s_add_u32 s32, s32, 0\n"
  "s_and_b32 s33, s32, 3\n"
  "s_lshl_b32 s33, s33, 5\n"
  "s_lshr_b32 s32, s32, 2\n"
  "s_add_u32 s33, s33, s32\n"
  "s_lshl_b32 s32, s28, 7\n"
  "s_add_u32 s33, s33, s32\n"
  "s_mul_i32 s32, s33, 1280\n"
  "s_lshl_b32 s34, s37, 7\n"
  "s_waitcnt lgkmcnt(0)\n"
  "s_add_u32 s52, s8, s34\n"
  "s_addc_u32 s53, s9, 0\n"
  "s_add_u32 m0, s40, 0\n"
  "v_add_u32 v8, s32, v2\n"
  "global_load_lds_dwordx4 v8, s[52:53]\n"
  "s_add_u32 s32, s32, 20480\n"
  "s_add_u32 m0, s40, 1024\n"
  "v_add_u32 v9, s32, v3\n"
  "global_load_lds_dwordx4 v9, s[52:53]\n"
  "s_mul_i32 s32, s24, 3\n"
  "s_add_u32 s32, s32, 1\n"
  "s_and_b32 s33, s32, 3\n"
  "s_lshl_b32 s33, s33, 5\n"
  "s_lshr_b32 s32, s32, 2\n"
  "s_add_u32 s33, s33, s32\n"
  "s_lshl_b32 s32, s28, 7\n"
  "s_add_u32 s33, s33, s32\n"
  "s_mul_i32 s32, s33, 1280\n"
  "s_add_u32 m0, s40, 2048\n"
  "v_add_u32 v10, s32, v2\n"
  "global_load_lds_dwordx4 v10, s[52:53]\n"
  "s_add_u32 s32, s32, 20480\n"
  "s_add_u32 m0, s40, 3072\n"
  "v_add_u32 v11, s32, v3\n"
  "global_load_lds_dwordx4 v11, s[52:53]\n"
  "s_mul_i32 s32, s24, 3\n"
  "s_add_u32 s32, s32, 2\n"
  "s_and_b32 s33, s32, 3\n"
  "s_lshl_b32 s33, s33, 5\n"
  "s_lshr_b32 s32, s32, 2\n"
  "s_add_u32 s33, s33, s32\n"
  "s_lshl_b32 s32, s28, 7\n"
  "s_add_u32 s33, s33, s32\n"
  "s_max_i32 s32, s32, 16\n"
  "s_sub_u32 s32, s32, 15\n"
  "s_cmp_eq_u32 s24, 2\n"
  "s_cselect_b32 s33, s32, s33\n"
  "s_mul_i32 s32, s33, 1280\n"
  "s_add_u32 m0, s40, 4096\n"
  "v_add_u32 v12, s32, v2\n"
  "global_load_lds_dwordx4 v12, s[52:53]\n"
  "s_movk_i32 s34, -1280\n"
  "s_cmp_eq_u32 s24, 2\n"
  "s_cselect_b32 s34, s34, 20480\n"
  "s_add_u32 s32, s32, s34\n"
  "s_add_u32 m0, s40, 5120\n"
  "v_add_u32 v13, s32, v3\n"
  "global_load_lds_dwordx4 v13, s[52:53]\n"
  "v_lshlrev_b32 v66, 4, v1\n"
  "s_cmp_lg_u32 s24, 0\n"
  "s_cbranch_scc1 .Lcom\n"
  "s_mov_b32 m0, 161792\n"
  "s_nop 0\n"
  "global_load_lds_dwordx4 v66, s[14:15]\n"
  "s_mov_b64 exec, 65535\n"
  "s_mov_b32 m0, 161792\n"
  "s_nop 0\n"
  "global_load_lds_dwordx4 v66, s[14:15] offset:1024\n"
  "s_mov_b64 exec, -1\n"
  "v_mov_b32 v2, 5312512\n"
  "global_load_dwordx2 v[64:65], v2, s[20:21] sc1\n"
  ".Lcom:\n"
  "s_add_u32 s37, s37, 1\n"
  "s_cmp_eq_u32 s37, 10\n"
  "s_cselect_b32 s32, s36, s35\n"
  "s_cselect_b32 s33, -1, 0\n"
  "s_cselect_b32 s37, 0, s37\n"
  "s_add_u32 s52, s52, s32\n"
  "s_addc_u32 s53, s53, s33\n"
  "s_add_u32 s40, s39, 49152\n"
  "s_lshr_b32 s30, s24, 2\n"
  "s_and_b32 s31, s24, 3\n"
  "v_lshrrev_b32 v6, 4, v1\n"
  "v_and_b32 v7, 15, v1\n"
  "v_lshrrev_b32 v2, 1, v7\n"
  "v_xor_b32 v2, v2, v6\n"
  "v_lshlrev_b32 v2, 4, v2\n"
  "v_lshl_add_u32 v2, v7, 7, v2\n"
  "s_lshl_b32 s6, s30, 13\n"
  "v_add_u32 v24, s6, v2\n"
  "v_xor_b32 v25, 64, v24\n"
  "s_and_b32 s6, s29, 3\n"
  "s_lshl_b32 s6, s6, 1\n"
  "s_add_u32 s6, s6, s30\n"
  "s_lshl_b32 s7, s28, 3\n"
  "s_add_u32 s7, s7, s6\n"
  "s_lshl_b32 s7, s7, 6\n"
  "s_add_u32 s7, s7, 5242880\n"
  "v_lshl_add_u32 v51, v7, 2, s7\n"
  "s_mov_b32 s7, 72\n"
  "s_bitcmp1_b32 s7, s24\n"
  "s_cselect_b32 s6, 8, s6\n"
  "s_lshl_b32 s6, s6, 11\n"
  "v_add_u32 v29, s6, v2\n"
  "v_xor_b32 v30, 64, v29\n"
  "v_lshrrev_b32 v2, 1, v6\n"
  "v_lshlrev_b32 v2, 4, v2\n"
  "v_and_b32 v3, 1, v6\n"
  "v_lshl_add_u32 v2, v3, 2, v2\n"
  "v_lshrrev_b32 v229, 2, v7\n"
  "v_add_u32 v2, v2, v229\n"
  "v_and_b32 v230, 3, v7\n"
  "v_mul_u32_u24 v62, 160, v2\n"
  "v_lshl_add_u32 v62, v230, 3, v62\n"
  "s_mul_i32 s6, s30, 20480\n"
  "v_add_u32 v62, s6, v62\n"
  "v_mul_u32_u24 v2, 480, v2\n"
  "v_lshl_add_u32 v3, v230, 3, v2\n"
  "s_mul_i32 s6, s31, 40\n"
  "s_add_u32 s6, s6, 18432\n"
  "v_add_u32 v26, s6, v3\n"
  "s_mul_i32 s6, s30, 40\n"
  "s_add_u32 s6, s6, 18432\n"
  "v_add_u32 v28, s6, v3\n"
  "s_lshl_b32 s6, s24, 2\n"
  "s_lshr_b32 s48, 304091394, s6\n"
  "s_and_b32 s48, s48, 3\n"
  "s_mul_i32 s6, s48, 160\n"
  "s_add_u32 s6, s6, 18464\n"
  "v_mul_u32_u24 v3, 40, v230\n"
  "v_add3_u32 v27, v2, v3, s6\n"
  "s_lshl_b32 s6, s38, 7\n"
  "s_add_u32 s6, s6, 161792\n"
  "v_lshl_add_u32 v31, v6, 4, s6\n"
  "s_mov_b32 s4, 49152\n"
  "s_movk_i32 s5, 128\n"
  "s_lshl_b32 s6, s28, 3\n"
  "s_lshl_b32 s7, s30, 2\n"
  "s_add_u32 s6, s6, s7\n"
  "s_lshl_b32 s7, s29, 2\n"
  "s_add_u32 m0, s40, 0\n"
  "s_nop 0\n"
  "global_load_lds_dwordx4 v8, s[52:53]\n"
  "s_add_u32 s7, s7, s31\n"
  "s_lshl_b32 s49, s6, 6\n"
  "s_add_u32 s49, s49, s7\n"
  "s_lshl_b32 s7, s49, 7\n"
  "s_add_u32 s7, s7, 4194304\n"
  "v_lshlrev_b32 v2, 13, v6\n"
  "v_lshl_add_u32 v2, v7, 3, v2\n"
  "v_add_u32 v61, s7, v2\n"
  "s_lshl_b32 s7, s49, 9\n"
  "v_lshrrev_b32 v2, 5, v1\n"
  "v_lshlrev_b32 v2, 16, v2\n"
  "v_and_b32 v3, 511, v66\n"
  "v_add3_u32 v60, v2, v3, s7\n"
  "s_add_u32 s60, s20, 32768\n"
  "s_addc_u32 s61, s21, 0\n"
  "v_mov_b64 v[232:233], -1\n"
  "v_mov_b64 v[234:235], -1\n"
  "s_movk_i32 s50, 168\n"
  "s_lshr_b32 s43, s27, 2\n"
  "s_mul_i32 s49, s43, 160\n"
  "s_cmp_lg_u32 s30, 0\n"
  "s_cselect_b32 s54, s18, s16\n"
  "s_cselect_b32 s55, s19, s17\n"
  "s_add_u32 s54, s54, s49\n"
  "s_addc_u32 s55, s55, 0\n"
  "s_mul_i32 s46, s24, 5120\n"
  "s_lshl_b32 s47, s27, 1\n"
  "s_and_b32 s47, s47, 6\n"
  "s_add_u32 s47, s47, s43\n"
  "s_lshl_b32 s47, s47, 1\n"
  "s_movk_i32 s49, 1280\n"
  "v_mul_u32_u24 v2, 6554, v1\n"
  "v_lshrrev_b32 v19, 16, v2\n"
  "v_mad_i32_i24 v3, v19, -10, v1\n"
  "s_add_u32 m0, s40, 1024\n"
  "s_nop 0\n"
  "global_load_lds_dwordx4 v9, s[52:53]\n"
  "v_lshlrev_b32 v3, 4, v3\n"
  "v_lshrrev_b32 v4, 4, v19\n"
  "v_lshl_add_u32 v5, v4, 2, v19\n"
  "v_mad_u32_u24 v14, v5, s49, v3\n"
  "v_bfe_u32 v2, v19, 3, 1\n"
  "v_lshl_add_u32 v4, v2, 1, v4\n"
  "v_bfe_u32 v2, v19, 2, 1\n"
  "v_lshl_add_u32 v4, v4, 1, v2\n"
  "v_and_b32 v2, 3, v19\n"
  "v_mad_u32_u24 v2, v4, 20, v2\n"
  "v_add_u32 v2, 16, v2\n"
  "v_mad_u32_u24 v19, v2, s49, v3\n"
  "v_add_u32 v5, 64, v1\n"
  "v_mul_u32_u24 v2, 6554, v5\n"
  "v_lshrrev_b32 v20, 16, v2\n"
  "v_mad_i32_i24 v3, v20, -10, v5\n"
  "v_lshlrev_b32 v3, 4, v3\n"
  "v_lshrrev_b32 v4, 4, v20\n"
  "v_lshl_add_u32 v5, v4, 2, v20\n"
  "v_mad_u32_u24 v15, v5, s49, v3\n"
  "v_bfe_u32 v2, v20, 3, 1\n"
  "v_lshl_add_u32 v4, v2, 1, v4\n"
  "v_bfe_u32 v2, v20, 2, 1\n"
  "v_lshl_add_u32 v4, v4, 1, v2\n"
  "v_and_b32 v2, 3, v20\n"
  "v_mad_u32_u24 v2, v4, 20, v2\n"
  "v_add_u32 v2, 16, v2\n"
  "v_mad_u32_u24 v20, v2, s49, v3\n"
  "v_add_u32 v5, 128, v1\n"
  "v_mul_u32_u24 v2, 6554, v5\n"
  "v_lshrrev_b32 v21, 16, v2\n"
  "v_mad_i32_i24 v3, v21, -10, v5\n"
  "v_lshlrev_b32 v3, 4, v3\n"
  "v_lshrrev_b32 v4, 4, v21\n"
  "s_add_u32 m0, s40, 2048\n"
  "s_nop 0\n"
  "global_load_lds_dwordx4 v10, s[52:53]\n"
  "v_lshl_add_u32 v5, v4, 2, v21\n"
  "v_mad_u32_u24 v16, v5, s49, v3\n"
  "v_bfe_u32 v2, v21, 3, 1\n"
  "v_lshl_add_u32 v4, v2, 1, v4\n"
  "v_bfe_u32 v2, v21, 2, 1\n"
  "v_lshl_add_u32 v4, v4, 1, v2\n"
  "v_and_b32 v2, 3, v21\n"
  "v_mad_u32_u24 v2, v4, 20, v2\n"
  "v_add_u32 v2, 16, v2\n"
  "v_mad_u32_u24 v21, v2, s49, v3\n"
  "v_add_u32 v5, 192, v1\n"
  "v_mul_u32_u24 v2, 6554, v5\n"
  "v_lshrrev_b32 v22, 16, v2\n"
  "v_mad_i32_i24 v3, v22, -10, v5\n"
  "v_lshlrev_b32 v3, 4, v3\n"
  "v_lshrrev_b32 v4, 4, v22\n"
  "v_lshl_add_u32 v5, v4, 2, v22\n"
  "v_mad_u32_u24 v17, v5, s49, v3\n"
  "v_bfe_u32 v2, v22, 3, 1\n"
  "v_lshl_add_u32 v4, v2, 1, v4\n"
  "v_bfe_u32 v2, v22, 2, 1\n"
  "v_lshl_add_u32 v4, v4, 1, v2\n"
  "v_and_b32 v2, 3, v22\n"
  "v_mad_u32_u24 v2, v4, 20, v2\n"
  "v_add_u32 v2, 16, v2\n"
  "v_mad_u32_u24 v22, v2, s49, v3\n"
  "v_add_u32 v5, 256, v1\n"
  "v_mul_u32_u24 v2, 6554, v5\n"
  "v_lshrrev_b32 v23, 16, v2\n"
  "v_mad_i32_i24 v3, v23, -10, v5\n"
  "v_lshlrev_b32 v3, 4, v3\n"
  "v_lshrrev_b32 v4, 4, v23\n"
  "v_lshl_add_u32 v5, v4, 2, v23\n"
  "v_mad_u32_u24 v18, v5, s49, v3\n"
  "s_add_u32 m0, s40, 3072\n"
  "s_nop 0\n"
  "global_load_lds_dwordx4 v11, s[52:53]\n"
  "v_bfe_u32 v2, v23, 3, 1\n"
  "v_lshl_add_u32 v4, v2, 1, v4\n"
  "v_bfe_u32 v2, v23, 2, 1\n"
  "v_lshl_add_u32 v4, v4, 1, v2\n"
  "v_and_b32 v2, 3, v23\n"
  "v_mad_u32_u24 v2, v4, 20, v2\n"
  "v_add_u32 v2, 16, v2\n"
  "v_mad_u32_u24 v23, v2, s49, v3\n"
  "s_add_u32 s51, s43, s27\n"
  "v_lshlrev_b32 v224, 3, v1\n"
  "v_add_u32 v224, 143360, v224\n"
  "v_xor_b32 v2, v6, v229\n"
  "v_lshlrev_b32 v2, 3, v2\n"
  "v_lshl_add_u32 v225, v7, 5, v2\n"
  "v_add_u32 v225, 143360, v225\n"
  "v_lshl_add_u32 v2, v6, 2, v229\n"
  "v_lshlrev_b32 v226, 5, v2\n"
  "v_add_u32 v226, 143360, v226\n"
  "s_lshl_b32 s6, s31, 9\n"
  "s_add_u32 s7, s6, 14336\n"
  "v_add_u32 v52, s7, v224\n"
  "s_add_u32 s7, s6, 16384\n"
  "v_add_u32 v47, s7, v225\n"
  "v_xor_b32 v2, v230, v6\n"
  "v_lshl_add_u32 v2, v2, 3, v226\n"
  "v_add_u32 v53, s7, v2\n"
  "s_lshl_b32 s6, s30, 9\n"
  "s_add_u32 s7, s6, 14336\n"
  "v_add_u32 v48, s7, v224\n"
  "s_add_u32 s7, s6, 16384\n"
  "v_add_u32 v49, s7, v225\n"
  "s_mul_i32 s49, s30, 2560\n"
  "s_add_u32 s7, s49, 4096\n"
  "v_add_u32 v54, s7, v224\n"
  "s_add_u32 m0, s40, 4096\n"
  "s_nop 0\n"
  "global_load_lds_dwordx4 v12, s[52:53]\n"
  "s_cmp_eq_u32 s48, 2\n"
  "s_cselect_b64 s[58:59], -1, 0\n"
  "v_cndmask_b32 v2, v224, v225, s[58:59]\n"
  "s_mul_i32 s6, s48, 5120\n"
  "s_add_u32 s7, s6, -1024\n"
  "v_add_u32 v50, s7, v2\n"
  "s_add_u32 s7, s7, s49\n"
  "s_add_u32 s7, s7, 512\n"
  "s_lshl_b32 s6, s30, 11\n"
  "s_cmp_eq_u32 s48, 0\n"
  "s_cselect_b32 s7, s6, s7\n"
  "v_add_u32 v46, s7, v2\n"
  "v_mov_b32 v67, 90112\n"
  "v_cmp_eq_u32 vcc, s31, v6\n"
  "v_add_u32 v2, s6, v224\n"
  "v_xor_b32 v3, s31, v6\n"
  "v_cndmask_b32 v55, v67, v2, vcc\n"
  "v_lshl_add_u32 v2, v3, 3, v226\n"
  "s_add_u32 s7, s49, 9216\n"
  "v_add_u32 v2, s7, v2\n"
  "v_cmp_eq_u32 vcc, 0, v230\n"
  "s_nop 1\n"
  "v_cndmask_b32 v56, v67, v2, vcc\n"
  "v_cmp_eq_u32 vcc, 1, v230\n"
  "v_add_u32 v2, 512, v2\n"
  "s_nop 0\n"
  "v_cndmask_b32 v57, v67, v2, vcc\n"
  "v_cmp_eq_u32 vcc, 2, v230\n"
  "v_add_u32 v2, 512, v2\n"
  "s_nop 0\n"
  "v_cndmask_b32 v58, v67, v2, vcc\n"
  "v_cmp_eq_u32 vcc, 3, v230\n"
  "v_add_u32 v2, 512, v2\n"
  "s_nop 0\n"
  "v_cndmask_b32 v59, v67, v2, vcc\n"
  "v_and_b32 v2, 3, v1\n"
  "v_lshl_add_u32 v67, v2, 9, v67\n"
  "v_lshlrev_b32 v2, 2, v6\n"
  "s_add_u32 m0, s40, 5120\n"
  "s_nop 0\n"
  "global_load_lds_dwordx4 v13, s[52:53]\n"
  "v_sub_u32 v227, v7, v2\n"
  "v_mov_b32 v228, 4048155338\n"
  "s_cmp_lg_u32 s28, 0\n"
  "s_cselect_b64 s[58:59], -1, 0\n"
  "s_cmp_lg_u32 s30, 0\n"
  "s_cselect_b32 s59, 0, s59\n"
  "s_cselect_b32 s49, 0, -8\n"
  "v_subrev_u32 v2, 0, v227\n"
  "v_cmp_gt_i32 s[62:63], s49, v2\n"
  "v_cmp_gt_u32 vcc, 8, v2\n"
  "v_cmp_gt_i32 s[64:65], -8, v2\n"
  "s_and_b64 s[62:63], s[62:63], s[58:59]\n"
  "v_cndmask_b32 v32, v228, 0, vcc\n"
  "v_cndmask_b32 v36, v228, 0, s[64:65]\n"
  "v_cndmask_b32 v40, v228, 0, s[62:63]\n"
  "v_subrev_u32 v2, 1, v227\n"
  "v_cmp_gt_i32 s[62:63], s49, v2\n"
  "v_cmp_gt_u32 vcc, 8, v2\n"
  "v_cmp_gt_i32 s[64:65], -8, v2\n"
  "s_and_b64 s[62:63], s[62:63], s[58:59]\n"
  "v_cndmask_b32 v33, v228, 0, vcc\n"
  "v_cndmask_b32 v37, v228, 0, s[64:65]\n"
  "v_cndmask_b32 v41, v228, 0, s[62:63]\n"
  "v_subrev_u32 v2, 2, v227\n"
  "v_cmp_gt_i32 s[62:63], s49, v2\n"
  "v_cmp_gt_u32 vcc, 8, v2\n"
  "v_cmp_gt_i32 s[64:65], -8, v2\n"
  "s_and_b64 s[62:63], s[62:63], s[58:59]\n"
  "v_cndmask_b32 v34, v228, 0, vcc\n"
  "v_cndmask_b32 v38, v228, 0, s[64:65]\n"
  "v_cndmask_b32 v42, v228, 0, s[62:63]\n"
  "v_subrev_u32 v2, 3, v227\n"
  "v_cmp_gt_i32 s[62:63], s49, v2\n"
  "v_cmp_gt_u32 vcc, 8, v2\n"
  "v_cmp_gt_i32 s[64:65], -8, v2\n"
  "s_and_b64 s[62:63], s[62:63], s[58:59]\n"
  "v_cndmask_b32 v35, v228, 0, vcc\n"
  "v_cndmask_b32 v39, v228, 0, s[64:65]\n"
  "v_cndmask_b32 v43, v228, 0, s[62:63]\n"
  "v_add_u32 v2, 1, v227\n"
  "v_med3_i32 v2, v2, 0, 4\n"
  "v_lshlrev_b32 v2, 3, v2\n"
  "v_sub_u32 v2, 32, v2\n"
  "v_lshrrev_b64 v[44:45], v2, -1\n"
  "v_lshrrev_b64 v[44:45], v2, v[44:45]\n"
  "s_lshl_b32 s6, s26, 1\n"
  "s_bfe_u32 s7, s27, 65537\n"
  "s_add_u32 s44, s6, s7\n"
  "v_lshlrev_b32 v63, 2, v7\n"
  "s_add_u32 s37, s37, 1\n"
  "s_cmp_eq_u32 s37, 10\n"
  "s_cselect_b32 s32, s36, s35\n"
  "s_cselect_b32 s33, -1, 0\n"
  "s_cselect_b32 s37, 0, s37\n"
  "s_add_u32 s52, s52, s32\n"
  "s_addc_u32 s53, s53, s33\n"
  "s_add_u32 s40, s39, 98304\n"
  "s_mov_b32 s41, 0\n"
  "s_mov_b32 s42, 7\n"
  "s_mov_b32 s33, 72\n"
  "s_bitcmp1_b32 s33, s24\n"
  "s_cbranch_scc1 .Lr2\n"
  "s_cmp_lt_u32 s29, 4\n"
  "s_cselect_b32 s33, 33, 0\n"
  "s_bitcmp1_b32 s33, s24\n"
  "s_cbranch_scc1 .Lr1\n"
  "s_waitcnt vmcnt(6)\n"
  "s_barrier\n"
  "ds_read_b128 v[136:139], v24\n"
  "ds_read_b64_tr_b16 v[152:153], v26\n"
  "ds_read_b64_tr_b16 v[154:155], v26 offset:3840\n"
  "ds_read_b128 v[140:143], v24 offset:2048\n"
  "ds_read_b128 v[144:147], v24 offset:4096\n"
  "ds_read_b128 v[148:151], v24 offset:6144\n"
  "ds_read_b64_tr_b16 v[156:157], v26 offset:160\n"
  "ds_read_b64_tr_b16 v[158:159], v26 offset:4000\n"
  "ds_read_b64_tr_b16 v[164:165], v27\n"
  "ds_read_b64_tr_b16 v[166:167], v27 offset:3840\n"
  "ds_read_b64_tr_b16 v[160:161], v26 offset:320\n"
  "ds_read_b64_tr_b16 v[162:163], v26 offset:4160\n"
  "s_setprio 1\n"
  "s_waitcnt lgkmcnt(9)\n"
  "v_mfma_f32_16x16x32_bf16 v[68:71], v[152:155], v[136:139], 0\n"
  "s_add_u32 m0, s40, 0\n"
  "ds_read_b128 v[180:183], v25\n"
  "global_load_lds_dwordx4 v8, s[52:53]\n"
  "s_waitcnt lgkmcnt(9)\n"
  "v_mfma_f32_16x16x32_bf16 v[72:75], v[152:155], v[140:143], 0\n"
  "ds_read_b64_tr_b16 v[196:197], v26 offset:15360\n"
  "s_waitcnt lgkmcnt(9)\n"
  "v_mfma_f32_16x16x32_bf16 v[76:79], v[152:155], v[144:147], 0\n"
  "ds_read_b64_tr_b16 v[198:199], v26 offset:19200\n"
  "s_waitcnt lgkmcnt(6)\n"
  "v_mfma_f32_16x16x32_bf16 v[80:83], v[152:155], v[148:151], 0\n"
  "s_add_u32 m0, s40, 1024\n"
  "ds_read_b128 v[184:187], v25 offset:2048\n"
  "global_load_lds_dwordx4 v9, s[52:53]\n"
  "v_mfma_f32_16x16x32_bf16 v[96:99], v[156:159], v[148:151], 0\n"
  "ds_read_b128 v[188:191], v25 offset:4096\n"
  "v_mfma_f32_16x16x32_bf16 v[92:95], v[156:159], v[144:147], 0\n"
  "ds_read_b128 v[192:195], v25 offset:6144\n"
  "v_mfma_f32_16x16x32_bf16 v[88:91], v[156:159], v[140:143], 0\n"
  "s_add_u32 m0, s40, 2048\n"
  "ds_read_b64_tr_b16 v[200:201], v26 offset:15520\n"
  "global_load_lds_dwordx4 v10, s[52:53]\n"
  "v_mfma_f32_16x16x32_bf16 v[84:87], v[156:159], v[136:139], 0\n"
  "ds_read_b64_tr_b16 v[202:203], v26 offset:19360\n"
  "s_setprio 0\n"
  "s_waitcnt lgkmcnt(4)\n"
  "v_mfma_f32_16x16x32_bf16 v[116:119], v[164:167], v[136:139], 0\n"
  "ds_read_b64_tr_b16 v[208:209], v27 offset:15360\n"
  "v_mfma_f32_16x16x32_bf16 v[120:123], v[164:167], v[140:143], 0\n"
  "s_add_u32 m0, s40, 3072\n"
  "ds_read_b64_tr_b16 v[210:211], v27 offset:19200\n"
  "global_load_lds_dwordx4 v11, s[52:53]\n"
  "v_mfma_f32_16x16x32_bf16 v[124:127], v[164:167], v[144:147], 0\n"
  "ds_read_b64_tr_b16 v[204:205], v26 offset:15680\n"
  "v_mfma_f32_16x16x32_bf16 v[128:131], v[164:167], v[148:151], 0\n"
  "ds_read_b64_tr_b16 v[206:207], v26 offset:19520\n"
  "v_mfma_f32_16x16x32_bf16 v[112:115], v[148:151], v[160:163], 0\n"
  "s_add_u32 m0, s40, 4096\n"
  "s_nop 0\n"
  "global_load_lds_dwordx4 v12, s[52:53]\n"
  "v_mfma_f32_16x16x32_bf16 v[108:111], v[144:147], v[160:163], 0\n"
  "v_mfma_f32_16x16x32_bf16 v[104:107], v[140:143], v[160:163], 0\n"
  "v_mfma_f32_16x16x32_bf16 v[100:103], v[136:139], v[160:163], 0\n"
  "s_add_u32 m0, s40, 5120\n"
  "s_nop 0\n"
  "global_load_lds_dwordx4 v13, s[52:53]\n"
  "s_add_u32 s40, s39, s41\n"
  "s_cmp_eq_u32 s41, 98304\n"
  "s_cselect_b32 s34, -98304, s4\n"
  "s_add_u32 s41, s41, s34\n"
  "v_add_u32 v24, s34, v24\n"
  "v_add_u32 v26, s34, v26\n"
  "v_add_u32 v27, s34, v27\n"
  "v_add_u32 v25, s34, v25\n"
  "s_add_u32 s37, s37, 1\n"
  "s_cmp_eq_u32 s37, 10\n"
  "s_cselect_b32 s32, s36, s35\n"
  "s_cselect_b32 s33, -1, 0\n"
  "s_cselect_b32 s37, 0, s37\n"
  "s_add_u32 s52, s52, s32\n"
  "s_addc_u32 s53, s53, s33\n"
  "s_waitcnt vmcnt(6) lgkmcnt(0)\n"
  ".Lk0:\n"
  "s_waitcnt vmcnt(7) lgkmcnt(0)\n"
  "s_barrier\n"
  "s_setprio 3\n"
  "v_mfma_f32_16x16x32_bf16 v[68:71], v[196:199], v[180:183], v[68:71]\n"
  "ds_read_b128 v[136:139], v24\n"
  "v_mfma_f32_16x16x32_bf16 v[72:75], v[196:199], v[184:187], v[72:75]\n"
  "s_add_u32 m0, s40, 0\n"
  "ds_read_b64_tr_b16 v[152:153], v26\n"
  "global_load_lds_dwordx4 v8, s[52:53]\n"
  "v_mfma_f32_16x16x32_bf16 v[76:79], v[196:199], v[188:191], v[76:79]\n"
  "ds_read_b64_tr_b16 v[154:155], v26 offset:3840\n"
  "v_mfma_f32_16x16x32_bf16 v[80:83], v[196:199], v[192:195], v[80:83]\n"
  "ds_read_b128 v[140:143], v24 offset:2048\n"
  "v_mfma_f32_16x16x32_bf16 v[96:99], v[200:203], v[192:195], v[96:99]\n"
  "ds_read_b128 v[144:147], v24 offset:4096\n"
  "v_mfma_f32_16x16x32_bf16 v[92:95], v[200:203], v[188:191], v[92:95]\n"
  "ds_read_b128 v[148:151], v24 offset:6144\n"
  "v_mfma_f32_16x16x32_bf16 v[88:91], v[200:203], v[184:187], v[88:91]\n"
  "s_add_u32 m0, s40, 1024\n"
  "ds_read_b64_tr_b16 v[156:157], v26 offset:160\n"
  "global_load_lds_dwordx4 v9, s[52:53]\n"
  "v_mfma_f32_16x16x32_bf16 v[84:87], v[200:203], v[180:183], v[84:87]\n"
  "ds_read_b64_tr_b16 v[158:159], v26 offset:4000\n"
  "s_setprio 2\n"
  "v_mfma_f32_16x16x32_bf16 v[116:119], v[208:211], v[180:183], v[116:119]\n"
  "ds_read_b64_tr_b16 v[164:165], v27\n"
  "v_mfma_f32_16x16x32_bf16 v[120:123], v[208:211], v[184:187], v[120:123]\n"
  "ds_read_b64_tr_b16 v[166:167], v27 offset:3840\n"
  "v_mfma_f32_16x16x32_bf16 v[124:127], v[208:211], v[188:191], v[124:127]\n"
  "ds_read_b64_tr_b16 v[160:161], v26 offset:320\n"
  "v_mfma_f32_16x16x32_bf16 v[128:131], v[208:211], v[192:195], v[128:131]\n"
  "s_add_u32 m0, s40, 2048\n"
  "ds_read_b64_tr_b16 v[162:163], v26 offset:4160\n"
  "global_load_lds_dwordx4 v10, s[52:53]\n"
  "v_mfma_f32_16x16x32_bf16 v[112:115], v[192:195], v[204:207], v[112:115]\n"
  "v_mfma_f32_16x16x32_bf16 v[108:111], v[188:191], v[204:207], v[108:111]\n"
  "v_mfma_f32_16x16x32_bf16 v[104:107], v[184:187], v[204:207], v[104:107]\n"
  "v_mfma_f32_16x16x32_bf16 v[100:103], v[180:183], v[204:207], v[100:103]\n"
  "s_setprio 1\n"
  "s_waitcnt lgkmcnt(1)\n"
  "v_mfma_f32_16x16x32_bf16 v[68:71], v[152:155], v[136:139], v[68:71]\n"
  "ds_read_b128 v[180:183], v25\n"
  "v_mfma_f32_16x16x32_bf16 v[72:75], v[152:155], v[140:143], v[72:75]\n"
  "s_add_u32 m0, s40, 3072\n"
  "ds_read_b64_tr_b16 v[196:197], v26 offset:15360\n"
  "global_load_lds_dwordx4 v11, s[52:53]\n"
  "v_mfma_f32_16x16x32_bf16 v[76:79], v[152:155], v[144:147], v[76:79]\n"
  "ds_read_b64_tr_b16 v[198:199], v26 offset:19200\n"
  "v_mfma_f32_16x16x32_bf16 v[80:83], v[152:155], v[148:151], v[80:83]\n"
  "ds_read_b128 v[184:187], v25 offset:2048\n"
  "v_mfma_f32_16x16x32_bf16 v[96:99], v[156:159], v[148:151], v[96:99]\n"
  "ds_read_b128 v[188:191], v25 offset:4096\n"
  "v_mfma_f32_16x16x32_bf16 v[92:95], v[156:159], v[144:147], v[92:95]\n"
  "ds_read_b128 v[192:195], v25 offset:6144\n"
  "v_mfma_f32_16x16x32_bf16 v[88:91], v[156:159], v[140:143], v[88:91]\n"
  "s_add_u32 m0, s40, 4096\n"
  "ds_read_b64_tr_b16 v[200:201], v26 offset:15520\n"
  "global_load_lds_dwordx4 v12, s[52:53]\n"
  "v_mfma_f32_16x16x32_bf16 v[84:87], v[156:159], v[136:139], v[84:87]\n"
  "ds_read_b64_tr_b16 v[202:203], v26 offset:19360\n"
  "s_setprio 0\n"
  "v_mfma_f32_16x16x32_bf16 v[116:119], v[164:167], v[136:139], v[116:119]\n"
  "ds_read_b64_tr_b16 v[208:209], v27 offset:15360\n"
  "v_mfma_f32_16x16x32_bf16 v[120:123], v[164:167], v[140:143], v[120:123]\n"
  "ds_read_b64_tr_b16 v[210:211], v27 offset:19200\n"
  "v_mfma_f32_16x16x32_bf16 v[124:127], v[164:167], v[144:147], v[124:127]\n"
  "ds_read_b64_tr_b16 v[204:205], v26 offset:15680\n"
  "v_mfma_f32_16x16x32_bf16 v[128:131], v[164:167], v[148:151], v[128:131]\n"
  "s_add_u32 m0, s40, 5120\n"
  "ds_read_b64_tr_b16 v[206:207], v26 offset:19520\n"
  "global_load_lds_dwordx4 v13, s[52:53]\n"
  "s_add_u32 s40, s39, s41\n"
  "s_cmp_eq_u32 s41, 98304\n"
  "s_cselect_b32 s34, -98304, s4\n"
  "s_add_u32 s41, s41, s34\n"
  "v_add_u32 v24, s34, v24\n"
  "v_add_u32 v26, s34, v26\n"
  "v_add_u32 v27, s34, v27\n"
  "v_add_u32 v25, s34, v25\n"
  "s_add_u32 s37, s37, 1\n"
  "s_cmp_eq_u32 s37, 10\n"
  "s_cselect_b32 s32, s36, s35\n"
  "s_cselect_b32 s33, -1, 0\n"
  "s_cselect_b32 s37, 0, s37\n"
  "s_add_u32 s52, s52, s32\n"
  "s_addc_u32 s53, s53, s33\n"
  "s_waitcnt lgkmcnt(2)\n"
  "v_mfma_f32_16x16x32_bf16 v[112:115], v[148:151], v[160:163], v[112:115]\n"
  "v_mfma_f32_16x16x32_bf16 v[108:111], v[144:147], v[160:163], v[108:111]\n"
  "v_mfma_f32_16x16x32_bf16 v[104:107], v[140:143], v[160:163], v[104:107]\n"
  "s_bitcmp1_b32 s50, s42\n"
  "s_cbranch_scc0 .Lnpz0\n"
  "s_cmp_lt_u32 s42, 4\n"
  "s_cbranch_scc1 .Lpzr0\n"
  "s_cmp_lt_u32 s42, 6\n"
  "s_cselect_b32 s58, s60, s20\n"
  "s_cselect_b32 s59, s61, s21\n"
  "global_store_dwordx4 v60, v[232:235], s[58:59] sc1\n"
  "s_branch .Lnpz0\n"
  ".Lpzr0:\n"
  "global_store_dwordx2 v61, v[232:233], s[20:21] sc1\n"
  ".Lnpz0:\n"
  "v_mfma_f32_16x16x32_bf16 v[100:103], v[136:139], v[160:163], v[100:103]\n"
  "s_sub_u32 s42, s42, 1\n"
  "s_cmp_lg_u32 s42, 0\n"
  "s_cbranch_scc1 .Lk0\n"
  "s_add_u32 s32, s31, s51\n"
  "s_and_b32 s32, s32, 3\n"
  "s_mul_i32 s32, s32, 204800\n"
  "s_add_u32 s56, s54, s32\n"
  "s_addc_u32 s57, s55, 0\n"
  "s_waitcnt vmcnt(6) lgkmcnt(0)\n"
  "s_barrier\n"
  "s_setprio 3\n"
  "v_mfma_f32_16x16x32_bf16 v[68:71], v[196:199], v[180:183], v[68:71]\n"
  "s_cmp_lg_u32 s24, 0\n"
  "s_cbranch_scc1 .Lnfl0\n"
  "v_readfirstlane_b32 s6, v64\n"
  "v_readfirstlane_b32 s7, v65\n"
  "s_mul_hi_u32 s63, s6, 1284865837\n"
  "s_mul_i32 s49, s6, 1481765933\n"
  "s_add_u32 s63, s63, s49\n"
  "s_mul_i32 s49, s7, 1284865837\n"
  "s_add_u32 s63, s63, s49\n"
  "s_mul_i32 s62, s6, 1284865837\n"
  "s_add_u32 s62, s62, 4150755663\n"
  "s_addc_u32 s63, s63, 335903614\n"
  "s_lshl_b32 s6, s28, 4\n"
  "s_add_u32 s6, s6, s29\n"
  "s_lshl_b32 s6, s6, 3\n"
  "s_add_u32 s6, s6, 5308416\n"
  "v_mov_b32 v2, s6\n"
  "v_mov_b32 v64, s62\n"
  "v_mov_b32 v65, s63\n"
  "s_mov_b64 exec, 1\n"
  "global_store_dwordx2 v2, v[64:65], s[20:21] sc1\n"
  "s_mov_b64 exec, -1\n"
  ".Lnfl0:\n"
  "ds_read_b128 v[136:139], v24\n"
  "v_mfma_f32_16x16x32_bf16 v[72:75], v[196:199], v[184:187], v[72:75]\n"
  "s_add_u32 m0, s46, 49152\n"
  "ds_read_b64_tr_b16 v[152:153], v26\n"
  "global_load_lds_dwordx4 v19, s[56:57]\n"
  "v_mfma_f32_16x16x32_bf16 v[76:79], v[196:199], v[188:191], v[76:79]\n"
  "ds_read_b64_tr_b16 v[154:155], v26 offset:3840\n"
  "v_mfma_f32_16x16x32_bf16 v[80:83], v[196:199], v[192:195], v[80:83]\n"
  "ds_read_b128 v[140:143], v24 offset:2048\n"
  "v_mfma_f32_16x16x32_bf16 v[96:99], v[200:203], v[192:195], v[96:99]\n"
  "ds_read_b128 v[144:147], v24 offset:4096\n"
  "v_mfma_f32_16x16x32_bf16 v[92:95], v[200:203], v[188:191], v[92:95]\n"
  "ds_read_b128 v[148:151], v24 offset:6144\n"
  "v_mfma_f32_16x16x32_bf16 v[88:91], v[200:203], v[184:187], v[88:91]\n"
  "s_add_u32 m0, s46, 50176\n"
  "ds_read_b64_tr_b16 v[156:157], v26 offset:160\n"
  "global_load_lds_dwordx4 v20, s[56:57]\n"
  "v_mfma_f32_16x16x32_bf16 v[84:87], v[200:203], v[180:183], v[84:87]\n"
  "ds_read_b64_tr_b16 v[158:159], v26 offset:4000\n"
  "s_setprio 2\n"
  "v_mfma_f32_16x16x32_bf16 v[116:119], v[208:211], v[180:183], v[116:119]\n"
  "ds_read_b64_tr_b16 v[164:165], v27\n"
  "v_mfma_f32_16x16x32_bf16 v[120:123], v[208:211], v[184:187], v[120:123]\n"
  "ds_read_b64_tr_b16 v[166:167], v27 offset:3840\n"
  "v_mfma_f32_16x16x32_bf16 v[124:127], v[208:211], v[188:191], v[124:127]\n"
  "ds_read_b64_tr_b16 v[160:161], v26 offset:320\n"
  "v_mfma_f32_16x16x32_bf16 v[128:131], v[208:211], v[192:195], v[128:131]\n"
  "s_add_u32 m0, s46, 51200\n"
  "ds_read_b64_tr_b16 v[162:163], v26 offset:4160\n"
  "global_load_lds_dwordx4 v21, s[56:57]\n"
  "v_mfma_f32_16x16x32_bf16 v[112:115], v[192:195], v[204:207], v[112:115]\n"
  "v_mfma_f32_16x16x32_bf16 v[108:111], v[188:191], v[204:207], v[108:111]\n"
  "v_mfma_f32_16x16x32_bf16 v[104:107], v[184:187], v[204:207], v[104:107]\n"
  "v_mfma_f32_16x16x32_bf16 v[100:103], v[180:183], v[204:207], v[100:103]\n"
  "s_setprio 1\n"
  "s_waitcnt lgkmcnt(1)\n"
  "v_mfma_f32_16x16x32_bf16 v[68:71], v[152:155], v[136:139], v[68:71]\n"
  "ds_read_b128 v[180:183], v25\n"
  "v_mfma_f32_16x16x32_bf16 v[72:75], v[152:155], v[140:143], v[72:75]\n"
  "s_add_u32 m0, s46, 52224\n"
  "ds_read_b64_tr_b16 v[196:197], v26 offset:15360\n"
  "global_load_lds_dwordx4 v22, s[56:57]\n"
  "v_mfma_f32_16x16x32_bf16 v[76:79], v[152:155], v[144:147], v[76:79]\n"
  "ds_read_b64_tr_b16 v[198:199], v26 offset:19200\n"
  "v_mfma_f32_16x16x32_bf16 v[80:83], v[152:155], v[148:151], v[80:83]\n"
  "ds_read_b128 v[184:187], v25 offset:2048\n"
  "v_mfma_f32_16x16x32_bf16 v[96:99], v[156:159], v[148:151], v[96:99]\n"
  "ds_read_b128 v[188:191], v25 offset:4096\n"
  "v_mfma_f32_16x16x32_bf16 v[92:95], v[156:159], v[144:147], v[92:95]\n"
  "ds_read_b128 v[192:195], v25 offset:6144\n"
  "v_mfma_f32_16x16x32_bf16 v[88:91], v[156:159], v[140:143], v[88:91]\n"
  "s_add_u32 m0, s46, 53248\n"
  "ds_read_b64_tr_b16 v[200:201], v26 offset:15520\n"
  "global_load_lds_dwordx4 v23, s[56:57]\n"
  "v_mfma_f32_16x16x32_bf16 v[84:87], v[156:159], v[136:139], v[84:87]\n"
  "ds_read_b64_tr_b16 v[202:203], v26 offset:19360\n"
  "s_setprio 0\n"
  "v_mfma_f32_16x16x32_bf16 v[116:119], v[164:167], v[136:139], v[116:119]\n"
  "ds_read_b64_tr_b16 v[208:209], v27 offset:15360\n"
  "v_mfma_f32_16x16x32_bf16 v[120:123], v[164:167], v[140:143], v[120:123]\n"
  "ds_read_b64_tr_b16 v[210:211], v27 offset:19200\n"
  "v_mfma_f32_16x16x32_bf16 v[124:127], v[164:167], v[144:147], v[124:127]\n"
  "ds_read_b64_tr_b16 v[204:205], v26 offset:15680\n"
  "v_mfma_f32_16x16x32_bf16 v[128:131], v[164:167], v[148:151], v[128:131]\n"
  "ds_read_b64_tr_b16 v[206:207], v26 offset:19520\n"
  "s_add_u32 s40, s39, s41\n"
  "s_cmp_eq_u32 s41, 98304\n"
  "s_cselect_b32 s34, -98304, s4\n"
  "s_add_u32 s41, s41, s34\n"
  "v_add_u32 v24, s34, v24\n"
  "v_add_u32 v26, s34, v26\n"
  "v_add_u32 v27, s34, v27\n"
  "v_add_u32 v25, s34, v25\n"
  "s_add_u32 s32, s31, s47\n"
  "s_and_b32 s32, s32, 15\n"
  "s_mul_i32 s32, s32, 51200\n"
  "s_add_u32 s56, s54, s32\n"
  "s_addc_u32 s57, s55, 0\n"
  "s_waitcnt lgkmcnt(3)\n"
  "v_mfma_f32_16x16x32_bf16 v[112:115], v[148:151], v[160:163], v[112:115]\n"
  "v_mfma_f32_16x16x32_bf16 v[108:111], v[144:147], v[160:163], v[108:111]\n"
  "v_mfma_f32_16x16x32_bf16 v[104:107], v[140:143], v[160:163], v[104:107]\n"
  "v_mfma_f32_16x16x32_bf16 v[100:103], v[136:139], v[160:163], v[100:103]\n"
  "s_waitcnt vmcnt(5) lgkmcnt(0)\n"
  "s_barrier\n"
  "s_setprio 3\n"
  "v_mfma_f32_16x16x32_bf16 v[68:71], v[196:199], v[180:183], v[68:71]\n"
  "ds_read_b128 v[136:139], v24\n"
  "v_mfma_f32_16x16x32_bf16 v[72:75], v[196:199], v[184:187], v[72:75]\n"
  "ds_read_b64_tr_b16 v[152:153], v26\n"
  "v_mfma_f32_16x16x32_bf16 v[76:79], v[196:199], v[188:191], v[76:79]\n"
  "ds_read_b64_tr_b16 v[154:155], v26 offset:3840\n"
  "v_mfma_f32_16x16x32_bf16 v[80:83], v[196:199], v[192:195], v[80:83]\n"
  "ds_read_b128 v[140:143], v24 offset:2048\n"
  "v_mfma_f32_16x16x32_bf16 v[96:99], v[200:203], v[192:195], v[96:99]\n"
  "ds_read_b128 v[144:147], v24 offset:4096\n"
  "v_mfma_f32_16x16x32_bf16 v[92:95], v[200:203], v[188:191], v[92:95]\n"
  "ds_read_b128 v[148:151], v24 offset:6144\n"
  "v_mfma_f32_16x16x32_bf16 v[88:91], v[200:203], v[184:187], v[88:91]\n"
  "ds_read_b64_tr_b16 v[156:157], v26 offset:160\n"
  "v_mfma_f32_16x16x32_bf16 v[84:87], v[200:203], v[180:183], v[84:87]\n"
  "ds_read_b64_tr_b16 v[158:159], v26 offset:4000\n"
  "v_mfma_f32_16x16x32_bf16 v[116:119], v[208:211], v[180:183], v[116:119]\n"
  "ds_read_b64_tr_b16 v[164:165], v27\n"
  "v_mfma_f32_16x16x32_bf16 v[120:123], v[208:211], v[184:187], v[120:123]\n"
  "ds_read_b64_tr_b16 v[166:167], v27 offset:3840\n"
  "v_mfma_f32_16x16x32_bf16 v[124:127], v[208:211], v[188:191], v[124:127]\n"
  "ds_read_b64_tr_b16 v[160:161], v26 offset:320\n"
  "v_mfma_f32_16x16x32_bf16 v[128:131], v[208:211], v[192:195], v[128:131]\n"
  "ds_read_b64_tr_b16 v[162:163], v26 offset:4160\n"
  "v_mfma_f32_16x16x32_bf16 v[112:115], v[192:195], v[204:207], v[112:115]\n"
  "v_mfma_f32_16x16x32_bf16 v[108:111], v[188:191], v[204:207], v[108:111]\n"
  "v_mfma_f32_16x16x32_bf16 v[104:107], v[184:187], v[204:207], v[104:107]\n"
  "v_mfma_f32_16x16x32_bf16 v[100:103], v[180:183], v[204:207], v[100:103]\n"
  "s_setprio 2\n"
  "s_waitcnt lgkmcnt(2)\n"
  "v_mfma_f32_16x16x32_bf16 v[68:71], v[152:155], v[136:139], v[68:71]\n"
  "ds_read_b64_tr_b16 v[208:209], v27 offset:15360\n"
  "v_mfma_f32_16x16x32_bf16 v[72:75], v[152:155], v[140:143], v[72:75]\n"
  "ds_read_b64_tr_b16 v[210:211], v27 offset:19200\n"
  "v_mfma_f32_16x16x32_bf16 v[76:79], v[152:155], v[144:147], v[76:79]\n"
  "ds_read_b128 v[180:183], v25\n"
  "v_mfma_f32_16x16x32_bf16 v[80:83], v[152:155], v[148:151], v[80:83]\n"
  "ds_read_b128 v[184:187], v25 offset:2048\n"
  "v_mfma_f32_16x16x32_bf16 v[96:99], v[156:159], v[148:151], v[96:99]\n"
  "ds_read_b128 v[188:191], v25 offset:4096\n"
  "v_mfma_f32_16x16x32_bf16 v[92:95], v[156:159], v[144:147], v[92:95]\n"
  "ds_read_b128 v[192:195], v25 offset:6144\n"
  "v_mfma_f32_16x16x32_bf16 v[88:91], v[156:159], v[140:143], v[88:91]\n"
  "ds_read_b64_tr_b16 v[196:197], v26 offset:15360\n"
  "v_mfma_f32_16x16x32_bf16 v[84:87], v[156:159], v[136:139], v[84:87]\n"
  "ds_read_b64_tr_b16 v[198:199], v26 offset:19200\n"
  "v_mfma_f32_16x16x32_bf16 v[116:119], v[164:167], v[136:139], v[116:119]\n"
  "s_add_u32 m0, s46, 102400\n"
  "ds_read_b64_tr_b16 v[200:201], v26 offset:15520\n"
  "global_load_lds_dwordx4 v14, s[56:57]\n"
  "v_mfma_f32_16x16x32_bf16 v[120:123], v[164:167], v[140:143], v[120:123]\n"
  "ds_read_b64_tr_b16 v[202:203], v26 offset:19360\n"
  "v_mfma_f32_16x16x32_bf16 v[124:127], v[164:167], v[144:147], v[124:127]\n"
  "ds_read_b64_tr_b16 v[204:205], v26 offset:15680\n"
  "v_mfma_f32_16x16x32_bf16 v[128:131], v[164:167], v[148:151], v[128:131]\n"
  "ds_read_b64_tr_b16 v[206:207], v26 offset:19520\n"
  "s_waitcnt lgkmcnt(5)\n"
  "v_mfma_f32_16x16x32_bf16 v[112:115], v[148:151], v[160:163], v[112:115]\n"
  "v_mfma_f32_16x16x32_bf16 v[108:111], v[144:147], v[160:163], v[108:111]\n"
  "s_add_u32 m0, s46, 103424\n"
  "s_nop 0\n"
  "global_load_lds_dwordx4 v15, s[56:57]\n"
  "v_mfma_f32_16x16x32_bf16 v[104:107], v[140:143], v[160:163], v[104:107]\n"
  "v_mfma_f32_16x16x32_bf16 v[100:103], v[136:139], v[160:163], v[100:103]\n"
  "s_setprio 1\n"
  "v_mfma_f32_16x16x32_bf16 v[116:119], v[208:211], v[180:183], v[116:119]\n"
  "v_mov_b64 v[2:3], 0\n"
  "ds_write_b64 v67, v[2:3]\n"
  "v_mfma_f32_16x16x32_bf16 v[120:123], v[208:211], v[184:187], v[120:123]\n"
  "v_mfma_f32_16x16x32_bf16 v[124:127], v[208:211], v[188:191], v[124:127]\n"
  "s_add_u32 m0, s46, 104448\n"
  "s_nop 0\n"
  "global_load_lds_dwordx4 v16, s[56:57]\n"
  "v_mfma_f32_16x16x32_bf16 v[128:131], v[208:211], v[192:195], v[128:131]\n"
  "s_waitcnt lgkmcnt(1)\n"
  "v_mfma_f32_16x16x32_bf16 v[68:71], v[196:199], v[180:183], v[68:71]\n"
  "v_cvt_pk_bf16_f32 v116, v116, v117\n"
  "v_cvt_pk_bf16_f32 v117, v118, v119\n"
  "ds_write_b64 v46, v[116:117]\n"
  "v_mfma_f32_16x16x32_bf16 v[84:87], v[200:203], v[180:183], v[84:87]\n"
  "v_cvt_pk_bf16_f32 v120, v120, v121\n"
  "v_cvt_pk_bf16_f32 v121, v122, v123\n"
  "ds_write_b64 v46, v[120:121] offset:512\n"
  "v_mfma_f32_16x16x32_bf16 v[100:103], v[180:183], v[204:207], v[100:103]\n"
  "v_cvt_pk_bf16_f32 v124, v124, v125\n"
  "v_cvt_pk_bf16_f32 v125, v126, v127\n"
  "ds_write_b64 v46, v[124:125] offset:1024\n"
  "v_mfma_f32_16x16x32_bf16 v[72:75], v[196:199], v[184:187], v[72:75]\n"
  "s_add_u32 m0, s46, 105472\n"
  "s_nop 0\n"
  "global_load_lds_dwordx4 v17, s[56:57]\n"
  "v_cvt_pk_bf16_f32 v128, v128, v129\n"
  "v_cvt_pk_bf16_f32 v129, v130, v131\n"
  "ds_write_b64 v46, v[128:129] offset:1536\n"
  "v_mfma_f32_16x16x32_bf16 v[88:91], v[200:203], v[184:187], v[88:91]\n"
  "v_cvt_pk_bf16_f32 v136, v68, v69\n"
  "v_cvt_pk_bf16_f32 v137, v70, v71\n"
  "v_mfma_f32_16x16x32_bf16 v[104:107], v[184:187], v[204:207], v[104:107]\n"
  "v_cvt_pk_bf16_f32 v156, v84, v85\n"
  "v_cvt_pk_bf16_f32 v157, v86, v87\n"
  "v_mfma_f32_16x16x32_bf16 v[76:79], v[196:199], v[188:191], v[76:79]\n"
  "s_waitcnt vmcnt(4) lgkmcnt(0)\n"
  "s_barrier\n"
  "s_setprio 0\n"
  "ds_read_b64 v[152:153], v52\n"
  "ds_read_b64 v[154:155], v54\n"
  "v_cvt_pk_bf16_f32 v226, v100, v101\n"
  "v_cvt_pk_bf16_f32 v227, v102, v103\n"
  "v_mfma_f32_16x16x32_bf16 v[92:95], v[200:203], v[188:191], v[92:95]\n"
  "ds_read_b64 v[138:139], v55\n"
  "ds_read_b64 v[158:159], v54 offset:512\n"
  "v_cvt_pk_bf16_f32 v140, v72, v73\n"
  "v_cvt_pk_bf16_f32 v141, v74, v75\n"
  "v_mfma_f32_16x16x32_bf16 v[108:111], v[188:191], v[204:207], v[108:111]\n"
  "ds_read_b64 v[142:143], v55 offset:512\n"
  "ds_read_b64 v[162:163], v54 offset:1024\n"
  "v_cvt_pk_bf16_f32 v160, v88, v89\n"
  "v_cvt_pk_bf16_f32 v161, v90, v91\n"
  "v_mfma_f32_16x16x32_bf16 v[80:83], v[196:199], v[192:195], v[80:83]\n"
  "ds_read_b64 v[146:147], v55 offset:1024\n"
  "ds_read_b64 v[166:167], v54 offset:1536\n"
  "v_cvt_pk_bf16_f32 v228, v104, v105\n"
  "v_cvt_pk_bf16_f32 v229, v106, v107\n"
  "v_mfma_f32_16x16x32_bf16 v[96:99], v[200:203], v[192:195], v[96:99]\n"
  "ds_read_b64 v[150:151], v55 offset:1536\n"
  "ds_read_b64 v[170:171], v54 offset:2048\n"
  "v_cvt_pk_bf16_f32 v144, v76, v77\n"
  "v_cvt_pk_bf16_f32 v145, v78, v79\n"
  "v_mfma_f32_16x16x32_bf16 v[112:115], v[192:195], v[204:207], v[112:115]\n"
  "ds_read_b64_tr_b16 v[224:225], v53\n"
  "v_cvt_pk_bf16_f32 v164, v92, v93\n"
  "v_cvt_pk_bf16_f32 v165, v94, v95\n"
  "v_cvt_pk_bf16_f32 v230, v108, v109\n"
  "v_cvt_pk_bf16_f32 v231, v110, v111\n"
  "v_cvt_pk_bf16_f32 v148, v80, v81\n"
  "v_cvt_pk_bf16_f32 v149, v82, v83\n"
  "v_cvt_pk_bf16_f32 v168, v96, v97\n"
  "v_cvt_pk_bf16_f32 v169, v98, v99\n"
  "v_cvt_pk_bf16_f32 v232, v112, v113\n"
  "v_cvt_pk_bf16_f32 v233, v114, v115\n"
  "s_branch .Latt\n"
  ".Lr1:\n"
  "s_waitcnt vmcnt(6)\n"
  "s_barrier\n"
  "ds_read_b128 v[136:139], v24\n"
  "ds_read_b64_tr_b16 v[152:153], v26\n"
  "ds_read_b64_tr_b16 v[154:155], v26 offset:3840\n"
  "ds_read_b128 v[140:143], v24 offset:2048\n"
  "ds_read_b128 v[144:147], v24 offset:4096\n"
  "ds_read_b128 v[148:151], v24 offset:6144\n"
  "ds_read_b64_tr_b16 v[156:157], v26 offset:160\n"
  "ds_read_b64_tr_b16 v[158:159], v26 offset:4000\n"
  "ds_read_b64_tr_b16 v[164:165], v27\n"
  "ds_read_b64_tr_b16 v[166:167], v27 offset:3840\n"
  "ds_read_b64_tr_b16 v[160:161], v26 offset:320\n"
  "ds_read_b64_tr_b16 v[162:163], v26 offset:4160\n"
  "ds_read_b128 v[168:171], v29\n"
  "ds_read_b128 v[172:175], v31\n"
  "s_mov_b64 exec, 65535\n"
  "global_store_dword v51, v232, s[20:21] sc1\n"
  "s_mov_b64 exec, -1\n"
  "s_setprio 1\n"
  "s_waitcnt lgkmcnt(11)\n"
  "v_mfma_f32_16x16x32_bf16 v[68:71], v[152:155], v[136:139], 0\n"
  "s_add_u32 m0, s40, 0\n"
  "ds_read_b128 v[180:183], v25\n"
  "global_load_lds_dwordx4 v8, s[52:53]\n"
  "s_waitcnt lgkmcnt(11)\n"
  "v_mfma_f32_16x16x32_bf16 v[72:75], v[152:155], v[140:143], 0\n"
  "ds_read_b64_tr_b16 v[196:197], v26 offset:15360\n"
  "s_waitcnt lgkmcnt(8)\n"
  "v_mfma_f32_16x16x32_bf16 v[76:79], v[152:155], v[144:147], 0\n"
  "ds_read_b64_tr_b16 v[198:199], v26 offset:19200\n"
  "v_mfma_f32_16x16x32_bf16 v[80:83], v[152:155], v[148:151], 0\n"
  "s_add_u32 m0, s40, 1024\n"
  "ds_read_b128 v[184:187], v25 offset:2048\n"
  "global_load_lds_dwordx4 v9, s[52:53]\n"
  "v_mfma_f32_16x16x32_bf16 v[96:99], v[156:159], v[148:151], 0\n"
  "ds_read_b128 v[188:191], v25 offset:4096\n"
  "v_mfma_f32_16x16x32_bf16 v[92:95], v[156:159], v[144:147], 0\n"
  "ds_read_b128 v[192:195], v25 offset:6144\n"
  "v_mfma_f32_16x16x32_bf16 v[88:91], v[156:159], v[140:143], 0\n"
  "s_add_u32 m0, s40, 2048\n"
  "ds_read_b64_tr_b16 v[200:201], v26 offset:15520\n"
  "global_load_lds_dwordx4 v10, s[52:53]\n"
  "v_mfma_f32_16x16x32_bf16 v[84:87], v[156:159], v[136:139], 0\n"
  "ds_read_b64_tr_b16 v[202:203], v26 offset:19360\n"
  "s_setprio 0\n"
  "s_waitcnt lgkmcnt(4)\n"
  "v_mfma_f32_16x16x32_bf16 v[116:119], v[164:167], v[136:139], 0\n"
  "ds_read_b64_tr_b16 v[208:209], v27 offset:15360\n"
  "v_mfma_f32_16x16x32_bf16 v[120:123], v[164:167], v[140:143], 0\n"
  "s_add_u32 m0, s40, 3072\n"
  "ds_read_b64_tr_b16 v[210:211], v27 offset:19200\n"
  "global_load_lds_dwordx4 v11, s[52:53]\n"
  "v_mfma_f32_16x16x32_bf16 v[124:127], v[164:167], v[144:147], 0\n"
  "ds_read_b64_tr_b16 v[204:205], v26 offset:15680\n"
  "v_mfma_f32_16x16x32_bf16 v[128:131], v[164:167], v[148:151], 0\n"
  "ds_read_b64_tr_b16 v[206:207], v26 offset:19520\n"
  "v_mfma_f32_16x16x32_bf16 v[112:115], v[148:151], v[160:163], 0\n"
  "s_add_u32 m0, s40, 4096\n"
  "ds_read_b128 v[212:215], v30\n"
  "global_load_lds_dwordx4 v12, s[52:53]\n"
  "v_mfma_f32_16x16x32_bf16 v[108:111], v[144:147], v[160:163], 0\n"
  "ds_read_b128 v[216:219], v31 offset:64\n"
  "v_mfma_f32_16x16x32_bf16 v[104:107], v[140:143], v[160:163], 0\n"
  "v_mfma_f32_16x16x32_bf16 v[100:103], v[136:139], v[160:163], 0\n"
  "s_add_u32 m0, s40, 5120\n"
  "s_nop 0\n"
  "global_load_lds_dwordx4 v13, s[52:53]\n"
  "s_add_u32 s40, s39, s41\n"
  "s_cmp_eq_u32 s41, 98304\n"
  "s_cselect_b32 s34, -98304, s4\n"
  "s_add_u32 s41, s41, s34\n"
  "v_add_u32 v24, s34, v24\n"
  "v_add_u32 v26, s34, v26\n"
  "v_add_u32 v27, s34, v27\n"
  "v_add_u32 v25, s34, v25\n"
  "v_add_u32 v29, s34, v29\n"
  "v_add_u32 v30, s34, v30\n"
  "s_add_u32 s38, s38, 1\n"
  "s_cmp_eq_u32 s38, 10\n"
  "s_cselect_b32 s33, -1152, s5\n"
  "s_cselect_b32 s38, 0, s38\n"
  "v_add_u32 v31, s33, v31\n"
  "s_add_u32 s37, s37, 1\n"
  "s_cmp_eq_u32 s37, 10\n"
  "s_cselect_b32 s32, s36, s35\n"
  "s_cselect_b32 s33, -1, 0\n"
  "s_cselect_b32 s37, 0, s37\n"
  "s_add_u32 s52, s52, s32\n"
  "s_addc_u32 s53, s53, s33\n"
  "v_mfma_f32_16x16x32_bf16 v[132:135], v[172:175], v[168:171], 0\n"
  "s_waitcnt vmcnt(6) lgkmcnt(0)\n"
  ".Lk1:\n"
  "s_waitcnt vmcnt(7) lgkmcnt(0)\n"
  "s_barrier\n"
  "s_setprio 3\n"
  "v_mfma_f32_16x16x32_bf16 v[68:71], v[196:199], v[180:183], v[68:71]\n"
  "ds_read_b128 v[136:139], v24\n"
  "v_mfma_f32_16x16x32_bf16 v[72:75], v[196:199], v[184:187], v[72:75]\n"
  "s_add_u32 m0, s40, 0\n"
  "ds_read_b64_tr_b16 v[152:153], v26\n"
  "global_load_lds_dwordx4 v8, s[52:53]\n"
  "v_mfma_f32_16x16x32_bf16 v[76:79], v[196:199], v[188:191], v[76:79]\n"
  "ds_read_b64_tr_b16 v[154:155], v26 offset:3840\n"
  "v_mfma_f32_16x16x32_bf16 v[80:83], v[196:199], v[192:195], v[80:83]\n"
  "ds_read_b128 v[140:143], v24 offset:2048\n"
  "v_mfma_f32_16x16x32_bf16 v[96:99], v[200:203], v[192:195], v[96:99]\n"
  "ds_read_b128 v[144:147], v24 offset:4096\n"
  "v_mfma_f32_16x16x32_bf16 v[92:95], v[200:203], v[188:191], v[92:95]\n"
  "ds_read_b128 v[148:151], v24 offset:6144\n"
  "v_mfma_f32_16x16x32_bf16 v[88:91], v[200:203], v[184:187], v[88:91]\n"
  "s_add_u32 m0, s40, 1024\n"
  "ds_read_b64_tr_b16 v[156:157], v26 offset:160\n"
  "global_load_lds_dwordx4 v9, s[52:53]\n"
  "v_mfma_f32_16x16x32_bf16 v[84:87], v[200:203], v[180:183], v[84:87]\n"
  "ds_read_b64_tr_b16 v[158:159], v26 offset:4000\n"
  "s_setprio 2\n"
  "v_mfma_f32_16x16x32_bf16 v[116:119], v[208:211], v[180:183], v[116:119]\n"
  "ds_read_b64_tr_b16 v[164:165], v27\n"
  "v_mfma_f32_16x16x32_bf16 v[120:123], v[208:211], v[184:187], v[120:123]\n"
  "ds_read_b64_tr_b16 v[166:167], v27 offset:3840\n"
  "v_mfma_f32_16x16x32_bf16 v[124:127], v[208:211], v[188:191], v[124:127]\n"
  "ds_read_b64_tr_b16 v[160:161], v26 offset:320\n"
  "v_mfma_f32_16x16x32_bf16 v[128:131], v[208:211], v[192:195], v[128:131]\n"
  "s_add_u32 m0, s40, 2048\n"
  "ds_read_b64_tr_b16 v[162:163], v26 offset:4160\n"
  "global_load_lds_dwordx4 v10, s[52:53]\n"
  "v_mfma_f32_16x16x32_bf16 v[112:115], v[192:195], v[204:207], v[112:115]\n"
  "ds_read_b128 v[168:171], v29\n"
  "v_mfma_f32_16x16x32_bf16 v[108:111], v[188:191], v[204:207], v[108:111]\n"
  "ds_read_b128 v[172:175], v31\n"
  "v_mfma_f32_16x16x32_bf16 v[104:107], v[184:187], v[204:207], v[104:107]\n"
  "v_mfma_f32_16x16x32_bf16 v[100:103], v[180:183], v[204:207], v[100:103]\n"
  "v_mfma_f32_16x16x32_bf16 v[132:135], v[216:219], v[212:215], v[132:135]\n"
  "s_setprio 1\n"
  "s_waitcnt lgkmcnt(2)\n"
  "v_mfma_f32_16x16x32_bf16 v[68:71], v[152:155], v[136:139], v[68:71]\n"
  "ds_read_b128 v[180:183], v25\n"
  "v_mfma_f32_16x16x32_bf16 v[72:75], v[152:155], v[140:143], v[72:75]\n"
  "s_add_u32 m0, s40, 3072\n"
  "ds_read_b64_tr_b16 v[196:197], v26 offset:15360\n"
  "global_load_lds_dwordx4 v11, s[52:53]\n"
  "v_mfma_f32_16x16x32_bf16 v[76:79], v[152:155], v[144:147], v[76:79]\n"
  "ds_read_b64_tr_b16 v[198:199], v26 offset:19200\n"
  "v_mfma_f32_16x16x32_bf16 v[80:83], v[152:155], v[148:151], v[80:83]\n"
  "ds_read_b128 v[184:187], v25 offset:2048\n"
  "v_mfma_f32_16x16x32_bf16 v[96:99], v[156:159], v[148:151], v[96:99]\n"
  "ds_read_b128 v[188:191], v25 offset:4096\n"
  "v_mfma_f32_16x16x32_bf16 v[92:95], v[156:159], v[144:147], v[92:95]\n"
  "ds_read_b128 v[192:195], v25 offset:6144\n"
  "v_mfma_f32_16x16x32_bf16 v[88:91], v[156:159], v[140:143], v[88:91]\n"
  "s_add_u32 m0, s40, 4096\n"
  "ds_read_b64_tr_b16 v[200:201], v26 offset:15520\n"
  "global_load_lds_dwordx4 v12, s[52:53]\n"
  "v_mfma_f32_16x16x32_bf16 v[84:87], v[156:159], v[136:139], v[84:87]\n"
  "ds_read_b64_tr_b16 v[202:203], v26 offset:19360\n"
  "s_setprio 0\n"
  "v_mfma_f32_16x16x32_bf16 v[116:119], v[164:167], v[136:139], v[116:119]\n"
  "ds_read_b64_tr_b16 v[208:209], v27 offset:15360\n"
  "v_mfma_f32_16x16x32_bf16 v[120:123], v[164:167], v[140:143], v[120:123]\n"
  "ds_read_b64_tr_b16 v[210:211], v27 offset:19200\n"
  "v_mfma_f32_16x16x32_bf16 v[124:127], v[164:167], v[144:147], v[124:127]\n"
  "ds_read_b64_tr_b16 v[204:205], v26 offset:15680\n"
  "v_mfma_f32_16x16x32_bf16 v[128:131], v[164:167], v[148:151], v[128:131]\n"
  "s_add_u32 m0, s40, 5120\n"
  "ds_read_b64_tr_b16 v[206:207], v26 offset:19520\n"
  "global_load_lds_dwordx4 v13, s[52:53]\n"
  "v_mfma_f32_16x16x32_bf16 v[112:115], v[148:151], v[160:163], v[112:115]\n"
  "ds_read_b128 v[212:215], v30\n"
  "v_mfma_f32_16x16x32_bf16 v[108:111], v[144:147], v[160:163], v[108:111]\n"
  "s_waitcnt lgkmcnt(11)\n"
  "ds_read_b128 v[216:219], v31 offset:64\n"
  "s_add_u32 s40, s39, s41\n"
  "s_cmp_eq_u32 s41, 98304\n"
  "s_cselect_b32 s34, -98304, s4\n"
  "s_add_u32 s41, s41, s34\n"
  "v_add_u32 v24, s34, v24\n"
  "v_add_u32 v26, s34, v26\n"
  "v_add_u32 v27, s34, v27\n"
  "v_add_u32 v25, s34, v25\n"
  "v_add_u32 v29, s34, v29\n"
  "v_add_u32 v30, s34, v30\n"
  "s_add_u32 s38, s38, 1\n"
  "s_cmp_eq_u32 s38, 10\n"
  "s_cselect_b32 s33, -1152, s5\n"
  "s_cselect_b32 s38, 0, s38\n"
  "v_add_u32 v31, s33, v31\n"
  "s_add_u32 s37, s37, 1\n"
  "s_cmp_eq_u32 s37, 10\n"
  "s_cselect_b32 s32, s36, s35\n"
  "s_cselect_b32 s33, -1, 0\n"
  "s_cselect_b32 s37, 0, s37\n"
  "s_add_u32 s52, s52, s32\n"
  "s_addc_u32 s53, s53, s33\n"
  "v_mfma_f32_16x16x32_bf16 v[104:107], v[140:143], v[160:163], v[104:107]\n"
  "s_bitcmp1_b32 s50, s42\n"
  "s_cbranch_scc0 .Lnpz1\n"
  "s_cmp_lt_u32 s42, 4\n"
  "s_cbranch_scc1 .Lpzr1\n"
  "s_cmp_lt_u32 s42, 6\n"
  "s_cselect_b32 s58, s60, s20\n"
  "s_cselect_b32 s59, s61, s21\n"
  "global_store_dwordx4 v60, v[232:235], s[58:59] sc1\n"
  "s_branch .Lnpz1\n"
  ".Lpzr1:\n"
  "global_store_dwordx2 v61, v[232:233], s[20:21] sc1\n"
  ".Lnpz1:\n"
  "v_mfma_f32_16x16x32_bf16 v[100:103], v[136:139], v[160:163], v[100:103]\n"
  "v_mfma_f32_16x16x32_bf16 v[132:135], v[172:175], v[168:171], v[132:135]\n"
  "s_sub_u32 s42, s42, 1\n"
  "s_cmp_lg_u32 s42, 0\n"
  "s_cbranch_scc1 .Lk1\n"
  "s_add_u32 s32, s31, s51\n"
  "s_and_b32 s32, s32, 3\n"
  "s_mul_i32 s32, s32, 204800\n"
  "s_add_u32 s56, s54, s32\n"
  "s_addc_u32 s57, s55, 0\n"
  "s_waitcnt vmcnt(6) lgkmcnt(0)\n"
  "s_barrier\n"
  "s_setprio 3\n"
  "v_mfma_f32_16x16x32_bf16 v[68:71], v[196:199], v[180:183], v[68:71]\n"
  "s_cmp_lg_u32 s24, 0\n"
  "s_cbranch_scc1 .Lnfl1\n"
  "v_readfirstlane_b32 s6, v64\n"
  "v_readfirstlane_b32 s7, v65\n"
  "s_mul_hi_u32 s63, s6, 1284865837\n"
  "s_mul_i32 s49, s6, 1481765933\n"
  "s_add_u32 s63, s63, s49\n"
  "s_mul_i32 s49, s7, 1284865837\n"
  "s_add_u32 s63, s63, s49\n"
  "s_mul_i32 s62, s6, 1284865837\n"
  "s_add_u32 s62, s62, 4150755663\n"
  "s_addc_u32 s63, s63, 335903614\n"
  "s_lshl_b32 s6, s28, 4\n"
  "s_add_u32 s6, s6, s29\n"
  "s_lshl_b32 s6, s6, 3\n"
  "s_add_u32 s6, s6, 5308416\n"
  "v_mov_b32 v2, s6\n"
  "v_mov_b32 v64, s62\n"
  "v_mov_b32 v65, s63\n"
  "s_mov_b64 exec, 1\n"
  "global_store_dwordx2 v2, v[64:65], s[20:21] sc1\n"
  "s_mov_b64 exec, -1\n"
  ".Lnfl1:\n"
  "ds_read_b128 v[136:139], v24\n"
  "v_mfma_f32_16x16x32_bf16 v[72:75], v[196:199], v[184:187], v[72:75]\n"
  "s_add_u32 m0, s46, 49152\n"
  "ds_read_b64_tr_b16 v[152:153], v26\n"
  "global_load_lds_dwordx4 v19, s[56:57]\n"
  "v_mfma_f32_16x16x32_bf16 v[76:79], v[196:199], v[188:191], v[76:79]\n"
  "ds_read_b64_tr_b16 v[154:155], v26 offset:3840\n"
  "v_mfma_f32_16x16x32_bf16 v[80:83], v[196:199], v[192:195], v[80:83]\n"
  "ds_read_b128 v[140:143], v24 offset:2048\n"
  "v_mfma_f32_16x16x32_bf16 v[96:99], v[200:203], v[192:195], v[96:99]\n"
  "ds_read_b128 v[144:147], v24 offset:4096\n"
  "v_mfma_f32_16x16x32_bf16 v[92:95], v[200:203], v[188:191], v[92:95]\n"
  "ds_read_b128 v[148:151], v24 offset:6144\n"
  "v_mfma_f32_16x16x32_bf16 v[88:91], v[200:203], v[184:187], v[88:91]\n"
  "s_add_u32 m0, s46, 50176\n"
  "ds_read_b64_tr_b16 v[156:157], v26 offset:160\n"
  "global_load_lds_dwordx4 v20, s[56:57]\n"
  "v_mfma_f32_16x16x32_bf16 v[84:87], v[200:203], v[180:183], v[84:87]\n"
  "ds_read_b64_tr_b16 v[158:159], v26 offset:4000\n"
  "s_setprio 2\n"
  "v_mfma_f32_16x16x32_bf16 v[116:119], v[208:211], v[180:183], v[116:119]\n"
  "ds_read_b64_tr_b16 v[164:165], v27\n"
  "v_mfma_f32_16x16x32_bf16 v[120:123], v[208:211], v[184:187], v[120:123]\n"
  "ds_read_b64_tr_b16 v[166:167], v27 offset:3840\n"
  "v_mfma_f32_16x16x32_bf16 v[124:127], v[208:211], v[188:191], v[124:127]\n"
  "ds_read_b64_tr_b16 v[160:161], v26 offset:320\n"
  "v_mfma_f32_16x16x32_bf16 v[128:131], v[208:211], v[192:195], v[128:131]\n"
  "s_add_u32 m0, s46, 51200\n"
  "ds_read_b64_tr_b16 v[162:163], v26 offset:4160\n"
  "global_load_lds_dwordx4 v21, s[56:57]\n"
  "v_mfma_f32_16x16x32_bf16 v[112:115], v[192:195], v[204:207], v[112:115]\n"
  "ds_read_b128 v[168:171], v29\n"
  "v_mfma_f32_16x16x32_bf16 v[108:111], v[188:191], v[204:207], v[108:111]\n"
  "ds_read_b128 v[172:175], v31\n"
  "v_mfma_f32_16x16x32_bf16 v[104:107], v[184:187], v[204:207], v[104:107]\n"
  "v_mfma_f32_16x16x32_bf16 v[100:103], v[180:183], v[204:207], v[100:103]\n"
  "v_mfma_f32_16x16x32_bf16 v[132:135], v[216:219], v[212:215], v[132:135]\n"
  "s_setprio 1\n"
  "s_waitcnt lgkmcnt(2)\n"
  "v_mfma_f32_16x16x32_bf16 v[68:71], v[152:155], v[136:139], v[68:71]\n"
  "ds_read_b128 v[180:183], v25\n"
  "v_mfma_f32_16x16x32_bf16 v[72:75], v[152:155], v[140:143], v[72:75]\n"
  "s_add_u32 m0, s46, 52224\n"
  "ds_read_b64_tr_b16 v[196:197], v26 offset:15360\n"
  "global_load_lds_dwordx4 v22, s[56:57]\n"
  "v_mfma_f32_16x16x32_bf16 v[76:79], v[152:155], v[144:147], v[76:79]\n"
  "ds_read_b64_tr_b16 v[198:199], v26 offset:19200\n"
  "v_mfma_f32_16x16x32_bf16 v[80:83], v[152:155], v[148:151], v[80:83]\n"
  "ds_read_b128 v[184:187], v25 offset:2048\n"
  "v_mfma_f32_16x16x32_bf16 v[96:99], v[156:159], v[148:151], v[96:99]\n"
  "ds_read_b128 v[188:191], v25 offset:4096\n"
  "v_mfma_f32_16x16x32_bf16 v[92:95], v[156:159], v[144:147], v[92:95]\n"
  "ds_read_b128 v[192:195], v25 offset:6144\n"
  "v_mfma_f32_16x16x32_bf16 v[88:91], v[156:159], v[140:143], v[88:91]\n"
  "s_add_u32 m0, s46, 53248\n"
  "ds_read_b64_tr_b16 v[200:201], v26 offset:15520\n"
  "global_load_lds_dwordx4 v23, s[56:57]\n"
  "v_mfma_f32_16x16x32_bf16 v[84:87], v[156:159], v[136:139], v[84:87]\n"
  "ds_read_b64_tr_b16 v[202:203], v26 offset:19360\n"
  "s_setprio 0\n"
  "v_mfma_f32_16x16x32_bf16 v[116:119], v[164:167], v[136:139], v[116:119]\n"
  "ds_read_b64_tr_b16 v[208:209], v27 offset:15360\n"
  "v_mfma_f32_16x16x32_bf16 v[120:123], v[164:167], v[140:143], v[120:123]\n"
  "ds_read_b64_tr_b16 v[210:211], v27 offset:19200\n"
  "v_mfma_f32_16x16x32_bf16 v[124:127], v[164:167], v[144:147], v[124:127]\n"
  "ds_read_b64_tr_b16 v[204:205], v26 offset:15680\n"
  "v_mfma_f32_16x16x32_bf16 v[128:131], v[164:167], v[148:151], v[128:131]\n"
  "ds_read_b64_tr_b16 v[206:207], v26 offset:19520\n"
  "v_mfma_f32_16x16x32_bf16 v[112:115], v[148:151], v[160:163], v[112:115]\n"
  "ds_read_b128 v[212:215], v30\n"
  "v_mfma_f32_16x16x32_bf16 v[108:111], v[144:147], v[160:163], v[108:111]\n"
  "s_waitcnt lgkmcnt(11)\n"
  "ds_read_b128 v[216:219], v31 offset:64\n"
  "s_add_u32 s40, s39, s41\n"
  "s_cmp_eq_u32 s41, 98304\n"
  "s_cselect_b32 s34, -98304, s4\n"
  "s_add_u32 s41, s41, s34\n"
  "v_add_u32 v24, s34, v24\n"
  "v_add_u32 v26, s34, v26\n"
  "v_add_u32 v27, s34, v27\n"
  "v_add_u32 v25, s34, v25\n"
  "v_add_u32 v29, s34, v29\n"
  "v_add_u32 v30, s34, v30\n"
  "s_add_u32 s38, s38, 1\n"
  "s_cmp_eq_u32 s38, 10\n"
  "s_cselect_b32 s33, -1152, s5\n"
  "s_cselect_b32 s38, 0, s38\n"
  "v_add_u32 v31, s33, v31\n"
  "s_add_u32 s32, s31, s47\n"
  "s_and_b32 s32, s32, 15\n"
  "s_mul_i32 s32, s32, 51200\n"
  "s_add_u32 s56, s54, s32\n"
  "s_addc_u32 s57, s55, 0\n"
  "v_mfma_f32_16x16x32_bf16 v[104:107], v[140:143], v[160:163], v[104:107]\n"
  "v_mfma_f32_16x16x32_bf16 v[100:103], v[136:139], v[160:163], v[100:103]\n"
  "v_mfma_f32_16x16x32_bf16 v[132:135], v[172:175], v[168:171], v[132:135]\n"
  "s_waitcnt vmcnt(5) lgkmcnt(0)\n"
  "s_barrier\n"
  "s_setprio 3\n"
  "v_mfma_f32_16x16x32_bf16 v[68:71], v[196:199], v[180:183], v[68:71]\n"
  "ds_read_b128 v[136:139], v24\n"
  "v_mfma_f32_16x16x32_bf16 v[72:75], v[196:199], v[184:187], v[72:75]\n"
  "ds_read_b64_tr_b16 v[152:153], v26\n"
  "v_mfma_f32_16x16x32_bf16 v[76:79], v[196:199], v[188:191], v[76:79]\n"
  "ds_read_b64_tr_b16 v[154:155], v26 offset:3840\n"
  "v_mfma_f32_16x16x32_bf16 v[80:83], v[196:199], v[192:195], v[80:83]\n"
  "ds_read_b128 v[140:143], v24 offset:2048\n"
  "v_mfma_f32_16x16x32_bf16 v[96:99], v[200:203], v[192:195], v[96:99]\n"
  "ds_read_b128 v[144:147], v24 offset:4096\n"
  "v_mfma_f32_16x16x32_bf16 v[92:95], v[200:203], v[188:191], v[92:95]\n"
  "ds_read_b128 v[148:151], v24 offset:6144\n"
  "v_mfma_f32_16x16x32_bf16 v[88:91], v[200:203], v[184:187], v[88:91]\n"
  "ds_read_b64_tr_b16 v[156:157], v26 offset:160\n"
  "v_mfma_f32_16x16x32_bf16 v[84:87], v[200:203], v[180:183], v[84:87]\n"
  "ds_read_b64_tr_b16 v[158:159], v26 offset:4000\n"
  "v_mfma_f32_16x16x32_bf16 v[116:119], v[208:211], v[180:183], v[116:119]\n"
  "ds_read_b64_tr_b16 v[164:165], v27\n"
  "v_mfma_f32_16x16x32_bf16 v[120:123], v[208:211], v[184:187], v[120:123]\n"
  "ds_read_b64_tr_b16 v[166:167], v27 offset:3840\n"
  "v_mfma_f32_16x16x32_bf16 v[124:127], v[208:211], v[188:191], v[124:127]\n"
  "ds_read_b64_tr_b16 v[160:161], v26 offset:320\n"
  "v_mfma_f32_16x16x32_bf16 v[128:131], v[208:211], v[192:195], v[128:131]\n"
  "ds_read_b64_tr_b16 v[162:163], v26 offset:4160\n"
  "v_mfma_f32_16x16x32_bf16 v[112:115], v[192:195], v[204:207], v[112:115]\n"
  "ds_read_b128 v[168:171], v29\n"
  "v_mfma_f32_16x16x32_bf16 v[108:111], v[188:191], v[204:207], v[108:111]\n"
  "ds_read_b128 v[172:175], v31\n"
  "v_mfma_f32_16x16x32_bf16 v[104:107], v[184:187], v[204:207], v[104:107]\n"
  "v_mfma_f32_16x16x32_bf16 v[100:103], v[180:183], v[204:207], v[100:103]\n"
  "v_mfma_f32_16x16x32_bf16 v[132:135], v[216:219], v[212:215], v[132:135]\n"
  "s_setprio 2\n"
  "s_waitcnt lgkmcnt(3)\n"
  "v_mfma_f32_16x16x32_bf16 v[68:71], v[152:155], v[136:139], v[68:71]\n"
  "ds_read_b64_tr_b16 v[208:209], v27 offset:15360\n"
  "v_mfma_f32_16x16x32_bf16 v[72:75], v[152:155], v[140:143], v[72:75]\n"
  "ds_read_b64_tr_b16 v[210:211], v27 offset:19200\n"
  "v_mfma_f32_16x16x32_bf16 v[76:79], v[152:155], v[144:147], v[76:79]\n"
  "ds_read_b128 v[180:183], v25\n"
  "v_mfma_f32_16x16x32_bf16 v[80:83], v[152:155], v[148:151], v[80:83]\n"
  "ds_read_b128 v[184:187], v25 offset:2048\n"
  "v_mfma_f32_16x16x32_bf16 v[96:99], v[156:159], v[148:151], v[96:99]\n"
  "ds_read_b128 v[188:191], v25 offset:4096\n"
  "v_mfma_f32_16x16x32_bf16 v[92:95], v[156:159], v[144:147], v[92:95]\n"
  "ds_read_b128 v[192:195], v25 offset:6144\n"
  "v_mfma_f32_16x16x32_bf16 v[88:91], v[156:159], v[140:143], v[88:91]\n"
  "ds_read_b128 v[212:215], v30\n"
  "v_mfma_f32_16x16x32_bf16 v[84:87], v[156:159], v[136:139], v[84:87]\n"
  "ds_read_b128 v[216:219], v31 offset:64\n"
  "v_mfma_f32_16x16x32_bf16 v[116:119], v[164:167], v[136:139], v[116:119]\n"
  "s_add_u32 m0, s46, 102400\n"
  "ds_read_b64_tr_b16 v[196:197], v26 offset:15360\n"
  "global_load_lds_dwordx4 v14, s[56:57]\n"
  "v_mfma_f32_16x16x32_bf16 v[120:123], v[164:167], v[140:143], v[120:123]\n"
  "ds_read_b64_tr_b16 v[198:199], v26 offset:19200\n"
  "v_mfma_f32_16x16x32_bf16 v[124:127], v[164:167], v[144:147], v[124:127]\n"
  "ds_read_b64_tr_b16 v[200:201], v26 offset:15520\n"
  "v_mfma_f32_16x16x32_bf16 v[128:131], v[164:167], v[148:151], v[128:131]\n"
  "ds_read_b64_tr_b16 v[202:203], v26 offset:19360\n"
  "s_waitcnt lgkmcnt(5)\n"
  "v_mfma_f32_16x16x32_bf16 v[112:115], v[148:151], v[160:163], v[112:115]\n"
  "ds_read_b64_tr_b16 v[204:205], v26 offset:15680\n"
  "v_mfma_f32_16x16x32_bf16 v[108:111], v[144:147], v[160:163], v[108:111]\n"
  "s_add_u32 m0, s46, 103424\n"
  "ds_read_b64_tr_b16 v[206:207], v26 offset:19520\n"
  "global_load_lds_dwordx4 v15, s[56:57]\n"
  "v_mfma_f32_16x16x32_bf16 v[104:107], v[140:143], v[160:163], v[104:107]\n"
  "v_mfma_f32_16x16x32_bf16 v[100:103], v[136:139], v[160:163], v[100:103]\n"
  "v_mfma_f32_16x16x32_bf16 v[132:135], v[172:175], v[168:171], v[132:135]\n"
  "s_setprio 1\n"
  "v_mfma_f32_16x16x32_bf16 v[116:119], v[208:211], v[180:183], v[116:119]\n"
  "v_mov_b64 v[2:3], 0\n"
  "ds_write_b64 v67, v[2:3]\n"
  "v_mfma_f32_16x16x32_bf16 v[120:123], v[208:211], v[184:187], v[120:123]\n"
  "v_mfma_f32_16x16x32_bf16 v[124:127], v[208:211], v[188:191], v[124:127]\n"
  "s_add_u32 m0, s46, 104448\n"
  "s_nop 0\n"
  "global_load_lds_dwordx4 v16, s[56:57]\n"
  "v_mfma_f32_16x16x32_bf16 v[128:131], v[208:211], v[192:195], v[128:131]\n"
  "s_waitcnt lgkmcnt(1)\n"
  "v_mfma_f32_16x16x32_bf16 v[132:135], v[216:219], v[212:215], v[132:135]\n"
  "v_cvt_pk_bf16_f32 v116, v116, v117\n"
  "v_cvt_pk_bf16_f32 v117, v118, v119\n"
  "ds_write_b64 v46, v[116:117]\n"
  "v_mfma_f32_16x16x32_bf16 v[68:71], v[196:199], v[180:183], v[68:71]\n"
  "v_cvt_pk_bf16_f32 v120, v120, v121\n"
  "v_cvt_pk_bf16_f32 v121, v122, v123\n"
  "ds_write_b64 v46, v[120:121] offset:512\n"
  "v_mfma_f32_16x16x32_bf16 v[84:87], v[200:203], v[180:183], v[84:87]\n"
  "v_cvt_pk_bf16_f32 v124, v124, v125\n"
  "v_cvt_pk_bf16_f32 v125, v126, v127\n"
  "ds_write_b64 v46, v[124:125] offset:1024\n"
  "v_mfma_f32_16x16x32_bf16 v[100:103], v[180:183], v[204:207], v[100:103]\n"
  "s_add_u32 m0, s46, 105472\n"
  "s_nop 0\n"
  "global_load_lds_dwordx4 v17, s[56:57]\n"
  "v_cvt_pk_bf16_f32 v128, v128, v129\n"
  "v_cvt_pk_bf16_f32 v129, v130, v131\n"
  "ds_write_b64 v46, v[128:129] offset:1536\n"
  "v_mfma_f32_16x16x32_bf16 v[72:75], v[196:199], v[184:187], v[72:75]\n"
  "v_cvt_pk_bf16_f32 v136, v68, v69\n"
  "v_cvt_pk_bf16_f32 v137, v70, v71\n"
  "v_mfma_f32_16x16x32_bf16 v[88:91], v[200:203], v[184:187], v[88:91]\n"
  "v_cvt_pk_bf16_f32 v156, v84, v85\n"
  "v_cvt_pk_bf16_f32 v157, v86, v87\n"
  "v_mfma_f32_16x16x32_bf16 v[104:107], v[184:187], v[204:207], v[104:107]\n"
  "s_waitcnt vmcnt(4) lgkmcnt(0)\n"
  "s_barrier\n"
  "s_setprio 0\n"
  "ds_read_b64 v[152:153], v52\n"
  "ds_read_b64 v[154:155], v54\n"
  "v_mul_f32 v2, 3216550459, v132\n"
  "v_exp_f32 v2, v2\n"
  "v_mfma_f32_16x16x32_bf16 v[76:79], v[196:199], v[188:191], v[76:79]\n"
  "ds_read_b64 v[138:139], v55\n"
  "ds_read_b64 v[158:159], v54 offset:512\n"
  "v_cvt_pk_bf16_f32 v226, v100, v101\n"
  "v_cvt_pk_bf16_f32 v227, v102, v103\n"
  "v_mfma_f32_16x16x32_bf16 v[92:95], v[200:203], v[188:191], v[92:95]\n"
  "ds_read_b64 v[142:143], v55 offset:512\n"
  "ds_read_b64 v[162:163], v54 offset:1024\n"
  "v_add_f32 v2, 1.0, v2\n"
  "v_rcp_f32 v2, v2\n"
  "v_mfma_f32_16x16x32_bf16 v[108:111], v[188:191], v[204:207], v[108:111]\n"
  "ds_read_b64 v[146:147], v55 offset:1024\n"
  "ds_read_b64 v[166:167], v54 offset:1536\n"
  "v_cvt_pk_bf16_f32 v140, v72, v73\n"
  "v_cvt_pk_bf16_f32 v141, v74, v75\n"
  "v_mfma_f32_16x16x32_bf16 v[80:83], v[196:199], v[192:195], v[80:83]\n"
  "ds_read_b64 v[150:151], v55 offset:1536\n"
  "ds_read_b64 v[170:171], v54 offset:2048\n"
  "s_mov_b64 exec, 65535\n"
  "global_store_dword v51, v2, s[20:21] sc1\n"
  "s_mov_b64 exec, -1\n"
  "v_mfma_f32_16x16x32_bf16 v[96:99], v[200:203], v[192:195], v[96:99]\n"
  "ds_read_b64_tr_b16 v[224:225], v53\n"
  "v_cvt_pk_bf16_f32 v160, v88, v89\n"
  "v_cvt_pk_bf16_f32 v161, v90, v91\n"
  "v_mfma_f32_16x16x32_bf16 v[112:115], v[192:195], v[204:207], v[112:115]\n"
  "v_cvt_pk_bf16_f32 v228, v104, v105\n"
  "v_cvt_pk_bf16_f32 v229, v106, v107\n"
  "v_cvt_pk_bf16_f32 v144, v76, v77\n"
  "v_cvt_pk_bf16_f32 v145, v78, v79\n"
  "v_cvt_pk_bf16_f32 v164, v92, v93\n"
  "v_cvt_pk_bf16_f32 v165, v94, v95\n"
  "v_cvt_pk_bf16_f32 v230, v108, v109\n"
  "v_cvt_pk_bf16_f32 v231, v110, v111\n"
  "v_cvt_pk_bf16_f32 v148, v80, v81\n"
  "v_cvt_pk_bf16_f32 v149, v82, v83\n"
  "v_cvt_pk_bf16_f32 v168, v96, v97\n"
  "v_cvt_pk_bf16_f32 v169, v98, v99\n"
  "v_cvt_pk_bf16_f32 v232, v112, v113\n"
  "v_cvt_pk_bf16_f32 v233, v114, v115\n"
  "s_branch .Latt\n"
  ".Lr2:\n"
  "s_waitcnt vmcnt(6)\n"
  "s_barrier\n"
  "ds_read_b128 v[136:139], v24\n"
  "ds_read_b64_tr_b16 v[152:153], v26\n"
  "ds_read_b64_tr_b16 v[154:155], v26 offset:3840\n"
  "ds_read_b128 v[140:143], v24 offset:2048\n"
  "ds_read_b128 v[144:147], v24 offset:4096\n"
  "ds_read_b128 v[148:151], v24 offset:6144\n"
  "ds_read_b64_tr_b16 v[160:161], v26 offset:320\n"
  "ds_read_b64_tr_b16 v[162:163], v26 offset:4160\n"
  "ds_read_b64_tr_b16 v[156:157], v26 offset:160\n"
  "ds_read_b64_tr_b16 v[158:159], v26 offset:4000\n"
  "ds_read_b128 v[168:171], v29\n"
  "ds_read_b64_tr_b16 v[172:173], v28 offset:160\n"
  "ds_read_b64_tr_b16 v[174:175], v28 offset:4000\n"
  "ds_read_b64_tr_b16 v[176:177], v28 offset:320\n"
  "ds_read_b64_tr_b16 v[178:179], v28 offset:4160\n"
  "s_waitcnt lgkmcnt(11)\n"
  "ds_read_b64_tr_b16 v[164:165], v27\n"
  "ds_read_b64_tr_b16 v[166:167], v27 offset:3840\n"
  "s_setprio 1\n"
  "v_mfma_f32_16x16x32_bf16 v[68:71], v[152:155], v[136:139], 0\n"
  "s_add_u32 m0, s40, 0\n"
  "ds_read_b128 v[180:183], v25\n"
  "global_load_lds_dwordx4 v8, s[52:53]\n"
  "v_mfma_f32_16x16x32_bf16 v[72:75], v[152:155], v[140:143], 0\n"
  "ds_read_b64_tr_b16 v[196:197], v26 offset:15360\n"
  "s_waitcnt lgkmcnt(12)\n"
  "v_mfma_f32_16x16x32_bf16 v[76:79], v[152:155], v[144:147], 0\n"
  "ds_read_b64_tr_b16 v[198:199], v26 offset:19200\n"
  "v_mfma_f32_16x16x32_bf16 v[80:83], v[152:155], v[148:151], 0\n"
  "s_add_u32 m0, s40, 1024\n"
  "ds_read_b128 v[184:187], v25 offset:2048\n"
  "global_load_lds_dwordx4 v9, s[52:53]\n"
  "s_waitcnt lgkmcnt(4)\n"
  "v_mfma_f32_16x16x32_bf16 v[112:115], v[148:151], v[160:163], 0\n"
  "ds_read_b128 v[188:191], v25 offset:4096\n"
  "v_mfma_f32_16x16x32_bf16 v[108:111], v[144:147], v[160:163], 0\n"
  "ds_read_b128 v[192:195], v25 offset:6144\n"
  "v_mfma_f32_16x16x32_bf16 v[104:107], v[140:143], v[160:163], 0\n"
  "s_add_u32 m0, s40, 2048\n"
  "ds_read_b64_tr_b16 v[204:205], v26 offset:15680\n"
  "global_load_lds_dwordx4 v10, s[52:53]\n"
  "v_mfma_f32_16x16x32_bf16 v[100:103], v[136:139], v[160:163], 0\n"
  "ds_read_b64_tr_b16 v[206:207], v26 offset:19520\n"
  "s_setprio 0\n"
  "v_mfma_f32_16x16x32_bf16 v[84:87], v[156:159], v[136:139], 0\n"
  "ds_read_b64_tr_b16 v[200:201], v26 offset:15520\n"
  "v_mfma_f32_16x16x32_bf16 v[88:91], v[156:159], v[140:143], 0\n"
  "s_add_u32 m0, s40, 3072\n"
  "ds_read_b64_tr_b16 v[202:203], v26 offset:19360\n"
  "global_load_lds_dwordx4 v11, s[52:53]\n"
  "v_mfma_f32_16x16x32_bf16 v[92:95], v[156:159], v[144:147], 0\n"
  "ds_read_b128 v[212:215], v30\n"
  "v_mfma_f32_16x16x32_bf16 v[96:99], v[156:159], v[148:151], 0\n"
  "ds_read_b64_tr_b16 v[216:217], v28 offset:15520\n"
  "v_mfma_f32_16x16x32_bf16 v[116:119], v[156:159], v[168:171], 0\n"
  "s_add_u32 m0, s40, 4096\n"
  "ds_read_b64_tr_b16 v[218:219], v28 offset:19360\n"
  "global_load_lds_dwordx4 v12, s[52:53]\n"
  "v_mfma_f32_16x16x32_bf16 v[120:123], v[160:163], v[168:171], 0\n"
  "ds_read_b64_tr_b16 v[220:221], v28 offset:15680\n"
  "v_mfma_f32_16x16x32_bf16 v[124:127], v[172:175], v[168:171], 0\n"
  "ds_read_b64_tr_b16 v[222:223], v28 offset:19520\n"
  "v_mfma_f32_16x16x32_bf16 v[128:131], v[176:179], v[168:171], 0\n"
  "s_add_u32 m0, s40, 5120\n"
  "s_waitcnt lgkmcnt(11)\n"
  "ds_read_b64_tr_b16 v[208:209], v27 offset:15360\n"
  "global_load_lds_dwordx4 v13, s[52:53]\n"
  "v_mfma_f32_16x16x32_bf16 v[132:135], v[164:167], v[168:171], 0\n"
  "ds_read_b64_tr_b16 v[210:211], v27 offset:19200\n"
  "s_add_u32 s40, s39, s41\n"
  "s_cmp_eq_u32 s41, 98304\n"
  "s_cselect_b32 s34, -98304, s4\n"
  "s_add_u32 s41, s41, s34\n"
  "v_add_u32 v24, s34, v24\n"
  "v_add_u32 v26, s34, v26\n"
  "v_add_u32 v27, s34, v27\n"
  "v_add_u32 v25, s34, v25\n"
  "v_add_u32 v29, s34, v29\n"
  "v_add_u32 v30, s34, v30\n"
  "v_add_u32 v28, s34, v28\n"
  "s_add_u32 s37, s37, 1\n"
  "s_cmp_eq_u32 s37, 10\n"
  "s_cselect_b32 s32, s36, s35\n"
  "s_cselect_b32 s33, -1, 0\n"
  "s_cselect_b32 s37, 0, s37\n"
  "s_add_u32 s52, s52, s32\n"
  "s_addc_u32 s53, s53, s33\n"
  "s_waitcnt vmcnt(6) lgkmcnt(0)\n"
  ".Lk2:\n"
  "s_waitcnt vmcnt(7) lgkmcnt(0)\n"
  "s_barrier\n"
  "s_setprio 3\n"
  "v_mfma_f32_16x16x32_bf16 v[68:71], v[196:199], v[180:183], v[68:71]\n"
  "ds_read_b128 v[136:139], v24\n"
  "v_mfma_f32_16x16x32_bf16 v[72:75], v[196:199], v[184:187], v[72:75]\n"
  "s_add_u32 m0, s40, 0\n"
  "ds_read_b64_tr_b16 v[152:153], v26\n"
  "global_load_lds_dwordx4 v8, s[52:53]\n"
  "v_mfma_f32_16x16x32_bf16 v[76:79], v[196:199], v[188:191], v[76:79]\n"
  "ds_read_b64_tr_b16 v[154:155], v26 offset:3840\n"
  "v_mfma_f32_16x16x32_bf16 v[80:83], v[196:199], v[192:195], v[80:83]\n"
  "ds_read_b128 v[140:143], v24 offset:2048\n"
  "v_mfma_f32_16x16x32_bf16 v[112:115], v[192:195], v[204:207], v[112:115]\n"
  "ds_read_b128 v[144:147], v24 offset:4096\n"
  "v_mfma_f32_16x16x32_bf16 v[108:111], v[188:191], v[204:207], v[108:111]\n"
  "ds_read_b128 v[148:151], v24 offset:6144\n"
  "v_mfma_f32_16x16x32_bf16 v[104:107], v[184:187], v[204:207], v[104:107]\n"
  "s_add_u32 m0, s40, 1024\n"
  "ds_read_b64_tr_b16 v[160:161], v26 offset:320\n"
  "global_load_lds_dwordx4 v9, s[52:53]\n"
  "v_mfma_f32_16x16x32_bf16 v[100:103], v[180:183], v[204:207], v[100:103]\n"
  "ds_read_b64_tr_b16 v[162:163], v26 offset:4160\n"
  "s_setprio 2\n"
  "v_mfma_f32_16x16x32_bf16 v[84:87], v[200:203], v[180:183], v[84:87]\n"
  "ds_read_b64_tr_b16 v[156:157], v26 offset:160\n"
  "v_mfma_f32_16x16x32_bf16 v[88:91], v[200:203], v[184:187], v[88:91]\n"
  "ds_read_b64_tr_b16 v[158:159], v26 offset:4000\n"
  "v_mfma_f32_16x16x32_bf16 v[92:95], v[200:203], v[188:191], v[92:95]\n"
  "ds_read_b128 v[168:171], v29\n"
  "v_mfma_f32_16x16x32_bf16 v[96:99], v[200:203], v[192:195], v[96:99]\n"
  "s_add_u32 m0, s40, 2048\n"
  "ds_read_b64_tr_b16 v[172:173], v28 offset:160\n"
  "global_load_lds_dwordx4 v10, s[52:53]\n"
  "v_mfma_f32_16x16x32_bf16 v[116:119], v[200:203], v[212:215], v[116:119]\n"
  "ds_read_b64_tr_b16 v[174:175], v28 offset:4000\n"
  "v_mfma_f32_16x16x32_bf16 v[120:123], v[204:207], v[212:215], v[120:123]\n"
  "ds_read_b64_tr_b16 v[176:177], v28 offset:320\n"
  "v_mfma_f32_16x16x32_bf16 v[124:127], v[216:219], v[212:215], v[124:127]\n"
  "ds_read_b64_tr_b16 v[178:179], v28 offset:4160\n"
  "v_mfma_f32_16x16x32_bf16 v[128:131], v[220:223], v[212:215], v[128:131]\n"
  "s_waitcnt lgkmcnt(11)\n"
  "ds_read_b64_tr_b16 v[164:165], v27\n"
  "v_mfma_f32_16x16x32_bf16 v[132:135], v[208:211], v[212:215], v[132:135]\n"
  "ds_read_b64_tr_b16 v[166:167], v27 offset:3840\n"
  "s_setprio 1\n"
  "v_mfma_f32_16x16x32_bf16 v[68:71], v[152:155], v[136:139], v[68:71]\n"
  "ds_read_b128 v[180:183], v25\n"
  "v_mfma_f32_16x16x32_bf16 v[72:75], v[152:155], v[140:143], v[72:75]\n"
  "s_add_u32 m0, s40, 3072\n"
  "ds_read_b64_tr_b16 v[196:197], v26 offset:15360\n"
  "global_load_lds_dwordx4 v11, s[52:53]\n"
  "s_waitcnt lgkmcnt(4)\n"
  "v_mfma_f32_16x16x32_bf16 v[76:79], v[152:155], v[144:147], v[76:79]\n"
  "ds_read_b64_tr_b16 v[198:199], v26 offset:19200\n"
  "v_mfma_f32_16x16x32_bf16 v[80:83], v[152:155], v[148:151], v[80:83]\n"
  "ds_read_b128 v[184:187], v25 offset:2048\n"
  "v_mfma_f32_16x16x32_bf16 v[112:115], v[148:151], v[160:163], v[112:115]\n"
  "ds_read_b128 v[188:191], v25 offset:4096\n"
  "v_mfma_f32_16x16x32_bf16 v[108:111], v[144:147], v[160:163], v[108:111]\n"
  "ds_read_b128 v[192:195], v25 offset:6144\n"
  "v_mfma_f32_16x16x32_bf16 v[104:107], v[140:143], v[160:163], v[104:107]\n"
  "s_add_u32 m0, s40, 4096\n"
  "ds_read_b64_tr_b16 v[204:205], v26 offset:15680\n"
  "global_load_lds_dwordx4 v12, s[52:53]\n"
  "v_mfma_f32_16x16x32_bf16 v[100:103], v[136:139], v[160:163], v[100:103]\n"
  "ds_read_b64_tr_b16 v[206:207], v26 offset:19520\n"
  "s_setprio 0\n"
  "v_mfma_f32_16x16x32_bf16 v[84:87], v[156:159], v[136:139], v[84:87]\n"
  "ds_read_b64_tr_b16 v[200:201], v26 offset:15520\n"
  "v_mfma_f32_16x16x32_bf16 v[88:91], v[156:159], v[140:143], v[88:91]\n"
  "ds_read_b64_tr_b16 v[202:203], v26 offset:19360\n"
  "v_mfma_f32_16x16x32_bf16 v[92:95], v[156:159], v[144:147], v[92:95]\n"
  "ds_read_b128 v[212:215], v30\n"
  "v_mfma_f32_16x16x32_bf16 v[96:99], v[156:159], v[148:151], v[96:99]\n"
  "s_add_u32 m0, s40, 5120\n"
  "ds_read_b64_tr_b16 v[216:217], v28 offset:15520\n"
  "global_load_lds_dwordx4 v13, s[52:53]\n"
  "v_mfma_f32_16x16x32_bf16 v[116:119], v[156:159], v[168:171], v[116:119]\n"
  "ds_read_b64_tr_b16 v[218:219], v28 offset:19360\n"
  "v_mfma_f32_16x16x32_bf16 v[120:123], v[160:163], v[168:171], v[120:123]\n"
  "s_waitcnt lgkmcnt(11)\n"
  "ds_read_b64_tr_b16 v[220:221], v28 offset:15680\n"
  "v_mfma_f32_16x16x32_bf16 v[124:127], v[172:175], v[168:171], v[124:127]\n"
  "s_bitcmp1_b32 s50, s42\n"
  "s_cbranch_scc0 .Lnpz2\n"
  "s_cmp_lt_u32 s42, 4\n"
  "s_cbranch_scc1 .Lpzr2\n"
  "s_cmp_lt_u32 s42, 6\n"
  "s_cselect_b32 s58, s60, s20\n"
  "s_cselect_b32 s59, s61, s21\n"
  "global_store_dwordx4 v60, v[232:235], s[58:59] sc1\n"
  "s_branch .Lnpz2\n"
  ".Lpzr2:\n"
  "global_store_dwordx2 v61, v[232:233], s[20:21] sc1\n"
  ".Lnpz2:\n"
  "ds_read_b64_tr_b16 v[222:223], v28 offset:19520\n"
  "v_mfma_f32_16x16x32_bf16 v[128:131], v[176:179], v[168:171], v[128:131]\n"
  "ds_read_b64_tr_b16 v[208:209], v27 offset:15360\n"
  "v_mfma_f32_16x16x32_bf16 v[132:135], v[164:167], v[168:171], v[132:135]\n"
  "ds_read_b64_tr_b16 v[210:211], v27 offset:19200\n"
  "s_add_u32 s40, s39, s41\n"
  "s_cmp_eq_u32 s41, 98304\n"
  "s_cselect_b32 s34, -98304, s4\n"
  "s_add_u32 s41, s41, s34\n"
  "v_add_u32 v24, s34, v24\n"
  "v_add_u32 v26, s34, v26\n"
  "v_add_u32 v27, s34, v27\n"
  "v_add_u32 v25, s34, v25\n"
  "v_add_u32 v29, s34, v29\n"
  "v_add_u32 v30, s34, v30\n"
  "v_add_u32 v28, s34, v28\n"
  "s_add_u32 s37, s37, 1\n"
  "s_cmp_eq_u32 s37, 10\n"
  "s_cselect_b32 s32, s36, s35\n"
  "s_cselect_b32 s33, -1, 0\n"
  "s_cselect_b32 s37, 0, s37\n"
  "s_add_u32 s52, s52, s32\n"
  "s_addc_u32 s53, s53, s33\n"
  "s_sub_u32 s42, s42, 1\n"
  "s_cmp_lg_u32 s42, 0\n"
  "s_cbranch_scc1 .Lk2\n"
  "s_add_u32 s32, s31, s51\n"
  "s_and_b32 s32, s32, 3\n"
  "s_mul_i32 s32, s32, 204800\n"
  "s_add_u32 s56, s54, s32\n"
  "s_addc_u32 s57, s55, 0\n"
  "s_waitcnt vmcnt(6) lgkmcnt(0)\n"
  "s_barrier\n"
  "s_setprio 3\n"
  "v_mfma_f32_16x16x32_bf16 v[68:71], v[196:199], v[180:183], v[68:71]\n"
  "ds_read_b128 v[136:139], v24\n"
  "v_mfma_f32_16x16x32_bf16 v[72:75], v[196:199], v[184:187], v[72:75]\n"
  "s_add_u32 m0, s46, 49152\n"
  "ds_read_b64_tr_b16 v[152:153], v26\n"
  "global_load_lds_dwordx4 v19, s[56:57]\n"
  "v_mfma_f32_16x16x32_bf16 v[76:79], v[196:199], v[188:191], v[76:79]\n"
  "ds_read_b64_tr_b16 v[154:155], v26 offset:3840\n"
  "v_mfma_f32_16x16x32_bf16 v[80:83], v[196:199], v[192:195], v[80:83]\n"
  "ds_read_b128 v[140:143], v24 offset:2048\n"
  "v_mfma_f32_16x16x32_bf16 v[112:115], v[192:195], v[204:207], v[112:115]\n"
  "ds_read_b128 v[144:147], v24 offset:4096\n"
  "v_mfma_f32_16x16x32_bf16 v[108:111], v[188:191], v[204:207], v[108:111]\n"
  "ds_read_b128 v[148:151], v24 offset:6144\n"
  "v_mfma_f32_16x16x32_bf16 v[104:107], v[184:187], v[204:207], v[104:107]\n"
  "s_add_u32 m0, s46, 50176\n"
  "ds_read_b64_tr_b16 v[160:161], v26 offset:320\n"
  "global_load_lds_dwordx4 v20, s[56:57]\n"
  "v_mfma_f32_16x16x32_bf16 v[100:103], v[180:183], v[204:207], v[100:103]\n"
  "ds_read_b64_tr_b16 v[162:163], v26 offset:4160\n"
  "s_setprio 2\n"
  "v_mfma_f32_16x16x32_bf16 v[84:87], v[200:203], v[180:183], v[84:87]\n"
  "ds_read_b64_tr_b16 v[156:157], v26 offset:160\n"
  "v_mfma_f32_16x16x32_bf16 v[88:91], v[200:203], v[184:187], v[88:91]\n"
  "ds_read_b64_tr_b16 v[158:159], v26 offset:4000\n"
  "v_mfma_f32_16x16x32_bf16 v[92:95], v[200:203], v[188:191], v[92:95]\n"
  "ds_read_b128 v[168:171], v29\n"
  "v_mfma_f32_16x16x32_bf16 v[96:99], v[200:203], v[192:195], v[96:99]\n"
  "s_add_u32 m0, s46, 51200\n"
  "ds_read_b64_tr_b16 v[172:173], v28 offset:160\n"
  "global_load_lds_dwordx4 v21, s[56:57]\n"
  "v_mfma_f32_16x16x32_bf16 v[116:119], v[200:203], v[212:215], v[116:119]\n"
  "ds_read_b64_tr_b16 v[174:175], v28 offset:4000\n"
  "v_mfma_f32_16x16x32_bf16 v[120:123], v[204:207], v[212:215], v[120:123]\n"
  "ds_read_b64_tr_b16 v[176:177], v28 offset:320\n"
  "v_mfma_f32_16x16x32_bf16 v[124:127], v[216:219], v[212:215], v[124:127]\n"
  "ds_read_b64_tr_b16 v[178:179], v28 offset:4160\n"
  "v_mfma_f32_16x16x32_bf16 v[128:131], v[220:223], v[212:215], v[128:131]\n"
  "s_waitcnt lgkmcnt(11)\n"
  "ds_read_b64_tr_b16 v[164:165], v27\n"
  "v_mfma_f32_16x16x32_bf16 v[132:135], v[208:211], v[212:215], v[132:135]\n"
  "ds_read_b64_tr_b16 v[166:167], v27 offset:3840\n"
  "s_setprio 1\n"
  "v_mfma_f32_16x16x32_bf16 v[68:71], v[152:155], v[136:139], v[68:71]\n"
  "ds_read_b128 v[180:183], v25\n"
  "v_mfma_f32_16x16x32_bf16 v[72:75], v[152:155], v[140:143], v[72:75]\n"
  "s_add_u32 m0, s46, 52224\n"
  "ds_read_b64_tr_b16 v[196:197], v26 offset:15360\n"
  "global_load_lds_dwordx4 v22, s[56:57]\n"
  "s_waitcnt lgkmcnt(4)\n"
  "v_mfma_f32_16x16x32_bf16 v[76:79], v[152:155], v[144:147], v[76:79]\n"
  "ds_read_b64_tr_b16 v[198:199], v26 offset:19200\n"
  "v_mfma_f32_16x16x32_bf16 v[80:83], v[152:155], v[148:151], v[80:83]\n"
  "ds_read_b128 v[184:187], v25 offset:2048\n"
  "v_mfma_f32_16x16x32_bf16 v[112:115], v[148:151], v[160:163], v[112:115]\n"
  "ds_read_b128 v[188:191], v25 offset:4096\n"
  "v_mfma_f32_16x16x32_bf16 v[108:111], v[144:147], v[160:163], v[108:111]\n"
  "ds_read_b128 v[192:195], v25 offset:6144\n"
  "v_mfma_f32_16x16x32_bf16 v[104:107], v[140:143], v[160:163], v[104:107]\n"
  "s_add_u32 m0, s46, 53248\n"
  "ds_read_b64_tr_b16 v[204:205], v26 offset:15680\n"
  "global_load_lds_dwordx4 v23, s[56:57]\n"
  "v_mfma_f32_16x16x32_bf16 v[100:103], v[136:139], v[160:163], v[100:103]\n"
  "ds_read_b64_tr_b16 v[206:207], v26 offset:19520\n"
  "s_setprio 0\n"
  "v_mfma_f32_16x16x32_bf16 v[84:87], v[156:159], v[136:139], v[84:87]\n"
  "ds_read_b64_tr_b16 v[200:201], v26 offset:15520\n"
  "v_mfma_f32_16x16x32_bf16 v[88:91], v[156:159], v[140:143], v[88:91]\n"
  "ds_read_b64_tr_b16 v[202:203], v26 offset:19360\n"
  "v_mfma_f32_16x16x32_bf16 v[92:95], v[156:159], v[144:147], v[92:95]\n"
  "ds_read_b128 v[212:215], v30\n"
  "v_mfma_f32_16x16x32_bf16 v[96:99], v[156:159], v[148:151], v[96:99]\n"
  "ds_read_b64_tr_b16 v[216:217], v28 offset:15520\n"
  "v_mfma_f32_16x16x32_bf16 v[116:119], v[156:159], v[168:171], v[116:119]\n"
  "ds_read_b64_tr_b16 v[218:219], v28 offset:19360\n"
  "v_mfma_f32_16x16x32_bf16 v[120:123], v[160:163], v[168:171], v[120:123]\n"
  "s_waitcnt lgkmcnt(11)\n"
  "ds_read_b64_tr_b16 v[220:221], v28 offset:15680\n"
  "v_mfma_f32_16x16x32_bf16 v[124:127], v[172:175], v[168:171], v[124:127]\n"
  "ds_read_b64_tr_b16 v[222:223], v28 offset:19520\n"
  "v_mfma_f32_16x16x32_bf16 v[128:131], v[176:179], v[168:171], v[128:131]\n"
  "ds_read_b64_tr_b16 v[208:209], v27 offset:15360\n"
  "v_mfma_f32_16x16x32_bf16 v[132:135], v[164:167], v[168:171], v[132:135]\n"
  "ds_read_b64_tr_b16 v[210:211], v27 offset:19200\n"
  "s_add_u32 s40, s39, s41\n"
  "s_cmp_eq_u32 s41, 98304\n"
  "s_cselect_b32 s34, -98304, s4\n"
  "s_add_u32 s41, s41, s34\n"
  "v_add_u32 v24, s34, v24\n"
  "v_add_u32 v26, s34, v26\n"
  "v_add_u32 v27, s34, v27\n"
  "v_add_u32 v25, s34, v25\n"
  "v_add_u32 v29, s34, v29\n"
  "v_add_u32 v30, s34, v30\n"
  "v_add_u32 v28, s34, v28\n"
  "s_add_u32 s32, s31, s47\n"
  "s_and_b32 s32, s32, 15\n"
  "s_mul_i32 s32, s32, 51200\n"
  "s_add_u32 s56, s54, s32\n"
  "s_addc_u32 s57, s55, 0\n"
  "s_waitcnt vmcnt(5) lgkmcnt(0)\n"
  "s_barrier\n"
  "s_setprio 3\n"
  "v_mfma_f32_16x16x32_bf16 v[68:71], v[196:199], v[180:183], v[68:71]\n"
  "ds_read_b128 v[136:139], v24\n"
  "v_mfma_f32_16x16x32_bf16 v[72:75], v[196:199], v[184:187], v[72:75]\n"
  "ds_read_b64_tr_b16 v[152:153], v26\n"
  "v_mfma_f32_16x16x32_bf16 v[76:79], v[196:199], v[188:191], v[76:79]\n"
  "ds_read_b64_tr_b16 v[154:155], v26 offset:3840\n"
  "v_mfma_f32_16x16x32_bf16 v[80:83], v[196:199], v[192:195], v[80:83]\n"
  "ds_read_b128 v[140:143], v24 offset:2048\n"
  "v_mfma_f32_16x16x32_bf16 v[112:115], v[192:195], v[204:207], v[112:115]\n"
  "ds_read_b128 v[144:147], v24 offset:4096\n"
  "v_mfma_f32_16x16x32_bf16 v[108:111], v[188:191], v[204:207], v[108:111]\n"
  "ds_read_b128 v[148:151], v24 offset:6144\n"
  "v_mfma_f32_16x16x32_bf16 v[104:107], v[184:187], v[204:207], v[104:107]\n"
  "ds_read_b64_tr_b16 v[160:161], v26 offset:320\n"
  "v_mfma_f32_16x16x32_bf16 v[100:103], v[180:183], v[204:207], v[100:103]\n"
  "ds_read_b64_tr_b16 v[162:163], v26 offset:4160\n"
  "v_mfma_f32_16x16x32_bf16 v[84:87], v[200:203], v[180:183], v[84:87]\n"
  "ds_read_b64_tr_b16 v[156:157], v26 offset:160\n"
  "v_mfma_f32_16x16x32_bf16 v[88:91], v[200:203], v[184:187], v[88:91]\n"
  "ds_read_b64_tr_b16 v[158:159], v26 offset:4000\n"
  "v_mfma_f32_16x16x32_bf16 v[92:95], v[200:203], v[188:191], v[92:95]\n"
  "ds_read_b128 v[168:171], v29\n"
  "v_mfma_f32_16x16x32_bf16 v[96:99], v[200:203], v[192:195], v[96:99]\n"
  "ds_read_b64_tr_b16 v[172:173], v28 offset:160\n"
  "v_mfma_f32_16x16x32_bf16 v[116:119], v[200:203], v[212:215], v[116:119]\n"
  "ds_read_b64_tr_b16 v[174:175], v28 offset:4000\n"
  "v_mfma_f32_16x16x32_bf16 v[120:123], v[204:207], v[212:215], v[120:123]\n"
  "ds_read_b64_tr_b16 v[176:177], v28 offset:320\n"
  "v_mfma_f32_16x16x32_bf16 v[124:127], v[216:219], v[212:215], v[124:127]\n"
  "ds_read_b64_tr_b16 v[178:179], v28 offset:4160\n"
  "v_mfma_f32_16x16x32_bf16 v[128:131], v[220:223], v[212:215], v[128:131]\n"
  "s_waitcnt lgkmcnt(11)\n"
  "ds_read_b64_tr_b16 v[164:165], v27\n"
  "v_mfma_f32_16x16x32_bf16 v[132:135], v[208:211], v[212:215], v[132:135]\n"
  "ds_read_b64_tr_b16 v[166:167], v27 offset:3840\n"
  "s_setprio 2\n"
  "v_mfma_f32_16x16x32_bf16 v[68:71], v[152:155], v[136:139], v[68:71]\n"
  "ds_read_b128 v[212:215], v30\n"
  "v_mfma_f32_16x16x32_bf16 v[72:75], v[152:155], v[140:143], v[72:75]\n"
  "ds_read_b64_tr_b16 v[200:201], v26 offset:15520\n"
  "s_waitcnt lgkmcnt(5)\n"
  "v_mfma_f32_16x16x32_bf16 v[76:79], v[152:155], v[144:147], v[76:79]\n"
  "ds_read_b64_tr_b16 v[202:203], v26 offset:19360\n"
  "v_mfma_f32_16x16x32_bf16 v[80:83], v[152:155], v[148:151], v[80:83]\n"
  "ds_read_b64_tr_b16 v[204:205], v26 offset:15680\n"
  "v_mfma_f32_16x16x32_bf16 v[112:115], v[148:151], v[160:163], v[112:115]\n"
  "ds_read_b64_tr_b16 v[206:207], v26 offset:19520\n"
  "v_mfma_f32_16x16x32_bf16 v[108:111], v[144:147], v[160:163], v[108:111]\n"
  "ds_read_b64_tr_b16 v[216:217], v28 offset:15520\n"
  "v_mfma_f32_16x16x32_bf16 v[104:107], v[140:143], v[160:163], v[104:107]\n"
  "ds_read_b64_tr_b16 v[218:219], v28 offset:19360\n"
  "v_mfma_f32_16x16x32_bf16 v[100:103], v[136:139], v[160:163], v[100:103]\n"
  "ds_read_b64_tr_b16 v[220:221], v28 offset:15680\n"
  "v_mfma_f32_16x16x32_bf16 v[84:87], v[156:159], v[136:139], v[84:87]\n"
  "s_add_u32 m0, s46, 102400\n"
  "ds_read_b64_tr_b16 v[222:223], v28 offset:19520\n"
  "global_load_lds_dwordx4 v14, s[56:57]\n"
  "v_mfma_f32_16x16x32_bf16 v[88:91], v[156:159], v[140:143], v[88:91]\n"
  "ds_read_b64_tr_b16 v[208:209], v27 offset:15360\n"
  "v_mfma_f32_16x16x32_bf16 v[92:95], v[156:159], v[144:147], v[92:95]\n"
  "ds_read_b64_tr_b16 v[210:211], v27 offset:19200\n"
  "v_mfma_f32_16x16x32_bf16 v[96:99], v[156:159], v[148:151], v[96:99]\n"
  "ds_read_b128 v[180:183], v25\n"
  "v_mfma_f32_16x16x32_bf16 v[116:119], v[156:159], v[168:171], v[116:119]\n"
  "s_waitcnt lgkmcnt(11)\n"
  "ds_read_b64_tr_b16 v[196:197], v26 offset:15360\n"
  "v_mfma_f32_16x16x32_bf16 v[120:123], v[160:163], v[168:171], v[120:123]\n"
  "s_add_u32 m0, s46, 103424\n"
  "ds_read_b64_tr_b16 v[198:199], v26 offset:19200\n"
  "global_load_lds_dwordx4 v15, s[56:57]\n"
  "v_mfma_f32_16x16x32_bf16 v[124:127], v[172:175], v[168:171], v[124:127]\n"
  "ds_read_b128 v[184:187], v25 offset:2048\n"
  "v_mfma_f32_16x16x32_bf16 v[128:131], v[176:179], v[168:171], v[128:131]\n"
  "ds_read_b128 v[188:191], v25 offset:4096\n"
  "v_mfma_f32_16x16x32_bf16 v[132:135], v[164:167], v[168:171], v[132:135]\n"
  "s_waitcnt lgkmcnt(11)\n"
  "ds_read_b128 v[192:195], v25 offset:6144\n"
  "s_setprio 1\n"
  "v_mfma_f32_16x16x32_bf16 v[116:119], v[200:203], v[212:215], v[116:119]\n"
  "v_mfma_f32_16x16x32_bf16 v[120:123], v[204:207], v[212:215], v[120:123]\n"
  "s_waitcnt lgkmcnt(3)\n"
  "v_mfma_f32_16x16x32_bf16 v[124:127], v[216:219], v[212:215], v[124:127]\n"
  "s_add_u32 m0, s46, 104448\n"
  "s_nop 0\n"
  "global_load_lds_dwordx4 v16, s[56:57]\n"
  "v_mfma_f32_16x16x32_bf16 v[128:131], v[220:223], v[212:215], v[128:131]\n"
  "v_mfma_f32_16x16x32_bf16 v[132:135], v[208:211], v[212:215], v[132:135]\n"
  "v_cvt_pk_bf16_f32 v116, v116, v117\n"
  "v_cvt_pk_bf16_f32 v117, v118, v119\n"
  "ds_write_b64 v52, v[116:117]\n"
  "v_mfma_f32_16x16x32_bf16 v[68:71], v[196:199], v[180:183], v[68:71]\n"
  "v_cvt_pk_bf16_f32 v120, v120, v121\n"
  "v_cvt_pk_bf16_f32 v121, v122, v123\n"
  "ds_write_b64 v47, v[120:121]\n"
  "v_mfma_f32_16x16x32_bf16 v[84:87], v[200:203], v[180:183], v[84:87]\n"
  "v_cvt_pk_bf16_f32 v124, v124, v125\n"
  "v_cvt_pk_bf16_f32 v125, v126, v127\n"
  "ds_write_b64 v48, v[124:125]\n"
  "v_mfma_f32_16x16x32_bf16 v[100:103], v[180:183], v[204:207], v[100:103]\n"
  "s_add_u32 m0, s46, 105472\n"
  "s_nop 0\n"
  "global_load_lds_dwordx4 v17, s[56:57]\n"
  "v_cvt_pk_bf16_f32 v128, v128, v129\n"
  "v_cvt_pk_bf16_f32 v129, v130, v131\n"
  "ds_write_b64 v49, v[128:129]\n"
  "s_waitcnt lgkmcnt(3)\n"
  "v_mfma_f32_16x16x32_bf16 v[72:75], v[196:199], v[184:187], v[72:75]\n"
  "v_cvt_pk_bf16_f32 v132, v132, v133\n"
  "v_cvt_pk_bf16_f32 v133, v134, v135\n"
  "ds_write_b64 v50, v[132:133]\n"
  "ds_write_b64 v50, v[132:133] offset:2560\n"
  "v_mfma_f32_16x16x32_bf16 v[88:91], v[200:203], v[184:187], v[88:91]\n"
  "v_cvt_pk_bf16_f32 v136, v68, v69\n"
  "v_cvt_pk_bf16_f32 v137, v70, v71\n"
  "v_mfma_f32_16x16x32_bf16 v[104:107], v[184:187], v[204:207], v[104:107]\n"
  "s_waitcnt vmcnt(4) lgkmcnt(0)\n"
  "s_barrier\n"
  "s_setprio 0\n"
  "ds_read_b64 v[152:153], v52\n"
  "ds_read_b64 v[154:155], v54\n"
  "v_cvt_pk_bf16_f32 v156, v84, v85\n"
  "v_cvt_pk_bf16_f32 v157, v86, v87\n"
  "v_mfma_f32_16x16x32_bf16 v[76:79], v[196:199], v[188:191], v[76:79]\n"
  "ds_read_b64 v[138:139], v55\n"
  "ds_read_b64 v[158:159], v54 offset:512\n"
  "v_cvt_pk_bf16_f32 v226, v100, v101\n"
  "v_cvt_pk_bf16_f32 v227, v102, v103\n"
  "v_mfma_f32_16x16x32_bf16 v[92:95], v[200:203], v[188:191], v[92:95]\n"
  "ds_read_b64 v[142:143], v55 offset:512\n"
  "ds_read_b64 v[162:163], v54 offset:1024\n"
  "v_cvt_pk_bf16_f32 v140, v72, v73\n"
  "v_cvt_pk_bf16_f32 v141, v74, v75\n"
  "v_mfma_f32_16x16x32_bf16 v[108:111], v[188:191], v[204:207], v[108:111]\n"
  "ds_read_b64 v[146:147], v55 offset:1024\n"
  "ds_read_b64 v[166:167], v54 offset:1536\n"
  "v_cvt_pk_bf16_f32 v160, v88, v89\n"
  "v_cvt_pk_bf16_f32 v161, v90, v91\n"
  "v_mfma_f32_16x16x32_bf16 v[80:83], v[196:199], v[192:195], v[80:83]\n"
  "ds_read_b64 v[150:151], v55 offset:1536\n"
  "ds_read_b64 v[170:171], v54 offset:2048\n"
  "v_cvt_pk_bf16_f32 v228, v104, v105\n"
  "v_cvt_pk_bf16_f32 v229, v106, v107\n"
  "v_mfma_f32_16x16x32_bf16 v[96:99], v[200:203], v[192:195], v[96:99]\n"
  "ds_read_b64_tr_b16 v[224:225], v53\n"
  "v_cvt_pk_bf16_f32 v144, v76, v77\n"
  "v_cvt_pk_bf16_f32 v145, v78, v79\n"
  "v_mfma_f32_16x16x32_bf16 v[112:115], v[192:195], v[204:207], v[112:115]\n"
  "v_cvt_pk_bf16_f32 v164, v92, v93\n"
  "v_cvt_pk_bf16_f32 v165, v94, v95\n"
  "v_cvt_pk_bf16_f32 v230, v108, v109\n"
  "v_cvt_pk_bf16_f32 v231, v110, v111\n"
  "v_cvt_pk_bf16_f32 v148, v80, v81\n"
  "v_cvt_pk_bf16_f32 v149, v82, v83\n"
  "v_cvt_pk_bf16_f32 v168, v96, v97\n"
  "v_cvt_pk_bf16_f32 v169, v98, v99\n"
  "v_cvt_pk_bf16_f32 v232, v112, v113\n"
  "v_cvt_pk_bf16_f32 v233, v114, v115\n"
  ".Latt:\n"
  "s_setprio 3\n"
  "s_lshl_b32 s32, s44, 7\n"
  "s_add_u32 s32, s32, 5308416\n"
  "v_lshl_add_u32 v9, v7, 3, s32\n"
  "v_add_u32 v8, 49152, v62\n"
  "s_waitcnt lgkmcnt(1)\n"
  "v_mfma_f32_16x16x32_bf16 v[100:103], v[156:159], v[136:139], v[32:35]\n"
  "v_mfma_f32_16x16x32_bf16 v[116:119], v[152:155], v[136:139], v[40:43]\n"
  "ds_read_b64_tr_b16 v[234:235], v56\n"
  "ds_read_b64_tr_b16 v[236:237], v56 offset:512\n"
  "s_add_u32 m0, s46, 106496\n"
  "s_nop 0\n"
  "global_load_lds_dwordx4 v18, s[56:57]\n"
  "v_mfma_f32_16x16x32_bf16 v[104:107], v[160:163], v[140:143], v[32:35]\n"
  "v_mfma_f32_16x16x32_bf16 v[120:123], v[156:159], v[140:143], v[36:39]\n"
  "ds_read_b64_tr_b16 v[238:239], v57\n"
  "ds_read_b64_tr_b16 v[240:241], v57 offset:512\n"
  "v_mfma_f32_16x16x32_bf16 v[108:111], v[164:167], v[144:147], v[32:35]\n"
  "v_mfma_f32_16x16x32_bf16 v[124:127], v[160:163], v[144:147], v[36:39]\n"
  "ds_read_b64_tr_b16 v[242:243], v58\n"
  "ds_read_b64_tr_b16 v[244:245], v58 offset:512\n"
  "v_mfma_f32_16x16x32_bf16 v[112:115], v[168:171], v[148:151], v[32:35]\n"
  "v_mfma_f32_16x16x32_bf16 v[128:131], v[164:167], v[148:151], v[36:39]\n"
  "ds_read_b64_tr_b16 v[246:247], v59\n"
  "ds_read_b64_tr_b16 v[248:249], v59 offset:512\n"
  "v_max_f32 v100, v100, v116\n"
  "v_max_f32 v101, v101, v117\n"
  "v_max_f32 v102, v102, v118\n"
  "v_max_f32 v103, v103, v119\n"
  "v_max3_f32 v250, v100, v101, v102\n"
  "v_max_f32 v250, v250, v103\n"
  "v_max_f32 v104, v104, v120\n"
  "v_max_f32 v105, v105, v121\n"
  "v_max_f32 v106, v106, v122\n"
  "v_max_f32 v107, v107, v123\n"
  "v_max3_f32 v252, v104, v105, v106\n"
  "v_max_f32 v252, v252, v107\n"
  "v_max_f32 v108, v108, v124\n"
  "v_max_f32 v109, v109, v125\n"
  "v_max_f32 v110, v110, v126\n"
  "v_max_f32 v111, v111, v127\n"
  "v_max3_f32 v2, v108, v109, v110\n"
  "v_max_f32 v2, v2, v111\n"
  "v_max_f32 v112, v112, v128\n"
  "v_max_f32 v113, v113, v129\n"
  "v_max_f32 v114, v114, v130\n"
  "v_max_f32 v115, v115, v131\n"
  "v_max3_f32 v4, v112, v113, v114\n"
  "v_max_f32 v4, v4, v115\n"
  "s_add_u32 s32, s31, s47\n"
  "s_add_u32 s32, s32, 4\n"
  "s_and_b32 s32, s32, 15\n"
  "s_mul_i32 s32, s32, 51200\n"
  "s_add_u32 s56, s54, s32\n"
  "s_addc_u32 s57, s55, 0\n"
  "s_add_u32 m0, s46, 0\n"
  "s_nop 0\n"
  "global_load_lds_dwordx4 v14, s[56:57]\n"
  "s_setprio 2\n"
  "s_mov_b32 s64, 1051011931\n"
  "ds_read_b64_tr_b16 v[136:137], v8\n"
  "v_permlane32_swap_b32 v250, v252\n"
  "v_permlane32_swap_b32 v2, v4\n"
  "v_max_f32 v254, v250, v252\n"
  "v_max_f32 v255, v2, v4\n"
  "ds_read_b64_tr_b16 v[138:139], v8 offset:1280\n"
  "ds_read_b64_tr_b16 v[140:141], v8 offset:32\n"
  "v_permlane16_swap_b32 v254, v255\n"
  "v_max_f32 v254, v254, v255\n"
  "v_mul_f32 v254, s64, v254\n"
  "ds_bpermute_b32 v250, v63, v254\n"
  "ds_bpermute_b32 v252, v63, v254 offset:128\n"
  "ds_bpermute_b32 v2, v63, v254 offset:64\n"
  "s_waitcnt lgkmcnt(11)\n"
  "ds_bpermute_b32 v4, v63, v254 offset:192\n"
  "ds_read_b64_tr_b16 v[142:143], v8 offset:1312\n"
  "ds_read_b64_tr_b16 v[144:145], v8 offset:64\n"
  "ds_read_b64_tr_b16 v[146:147], v8 offset:1344\n"
  "s_waitcnt lgkmcnt(6)\n"
  "v_pk_fma_f32 v[100:101], v[100:101], s[64:65], v[250:251] op_sel_hi:[1,0,0] neg_lo:[0,0,1] neg_hi:[0,0,1]\n"
  "v_pk_fma_f32 v[102:103], v[102:103], s[64:65], v[250:251] op_sel_hi:[1,0,0] neg_lo:[0,0,1] neg_hi:[0,0,1]\n"
  "v_exp_f32 v100, v100\n"
  "v_exp_f32 v101, v101\n"
  "v_exp_f32 v102, v102\n"
  "v_exp_f32 v103, v103\n"
  "s_waitcnt lgkmcnt(5)\n"
  "v_pk_fma_f32 v[104:105], v[104:105], s[64:65], v[252:253] op_sel_hi:[1,0,0] neg_lo:[0,0,1] neg_hi:[0,0,1]\n"
  "v_pk_fma_f32 v[106:107], v[106:107], s[64:65], v[252:253] op_sel_hi:[1,0,0] neg_lo:[0,0,1] neg_hi:[0,0,1]\n"
  "v_exp_f32 v104, v104\n"
  "v_exp_f32 v105, v105\n"
  "v_exp_f32 v106, v106\n"
  "v_exp_f32 v107, v107\n"
  "s_add_u32 m0, s46, 1024\n"
  "s_nop 0\n"
  "global_load_lds_dwordx4 v15, s[56:57]\n"
  "s_waitcnt lgkmcnt(4)\n"
  "v_pk_fma_f32 v[108:109], v[108:109], s[64:65], v[2:3] op_sel_hi:[1,0,0] neg_lo:[0,0,1] neg_hi:[0,0,1]\n"
  "v_pk_fma_f32 v[110:111], v[110:111], s[64:65], v[2:3] op_sel_hi:[1,0,0] neg_lo:[0,0,1] neg_hi:[0,0,1]\n"
  "v_exp_f32 v108, v108\n"
  "v_exp_f32 v109, v109\n"
  "v_exp_f32 v110, v110\n"
  "v_exp_f32 v111, v111\n"
  "s_waitcnt lgkmcnt(0)\n"
  "v_pk_fma_f32 v[112:113], v[112:113], s[64:65], v[4:5] op_sel_hi:[1,0,0] neg_lo:[0,0,1] neg_hi:[0,0,1]\n"
  "v_pk_fma_f32 v[114:115], v[114:115], s[64:65], v[4:5] op_sel_hi:[1,0,0] neg_lo:[0,0,1] neg_hi:[0,0,1]\n"
  "v_exp_f32 v112, v112\n"
  "v_exp_f32 v113, v113\n"
  "v_exp_f32 v114, v114\n"
  "v_exp_f32 v115, v115\n"
  "s_setprio 1\n"
  "v_pk_add_f32 v[116:117], v[100:101], v[102:103]\n"
  "v_pk_add_f32 v[120:121], v[104:105], v[106:107]\n"
  "v_pk_add_f32 v[124:125], v[108:109], v[110:111]\n"
  "v_pk_add_f32 v[128:129], v[112:113], v[114:115]\n"
  "v_add_f32 v250, v116, v117\n"
  "v_add_f32 v252, v120, v121\n"
  "v_add_f32 v2, v124, v125\n"
  "v_add_f32 v4, v128, v129\n"
  "ds_read_b64_tr_b16 v[148:149], v8 offset:96\n"
  "v_permlane32_swap_b32 v250, v252\n"
  "v_permlane32_swap_b32 v2, v4\n"
  "v_add_f32 v254, v250, v252\n"
  "v_add_f32 v255, v2, v4\n"
  "ds_read_b64_tr_b16 v[150:151], v8 offset:1376\n"
  "ds_read_b64_tr_b16 v[152:153], v8 offset:128\n"
  "v_permlane16_swap_b32 v254, v255\n"
  "v_add_f32 v254, v254, v255\n"
  "v_rcp_f32 v254, v254\n"
  "s_nop 0\n"
  "ds_bpermute_b32 v250, v63, v254\n"
  "ds_bpermute_b32 v252, v63, v254 offset:128\n"
  "ds_bpermute_b32 v2, v63, v254 offset:64\n"
  "ds_bpermute_b32 v4, v63, v254 offset:192\n"
  "ds_read_b64_tr_b16 v[154:155], v8 offset:1408\n"
  "ds_read_b64_tr_b16 v[156:157], v8 offset:5120\n"
  "ds_read_b64_tr_b16 v[158:159], v8 offset:6400\n"
  "s_add_u32 m0, s46, 2048\n"
  "s_nop 0\n"
  "global_load_lds_dwordx4 v16, s[56:57]\n"
  "s_setprio 0\n"
  "s_waitcnt lgkmcnt(6)\n"
  "v_pk_mul_f32 v[100:101], v[100:101], v[250:251] op_sel_hi:[1,0]\n"
  "v_pk_mul_f32 v[102:103], v[102:103], v[250:251] op_sel_hi:[1,0]\n"
  "v_cvt_pk_bf16_f32 v100, v100, v101\n"
  "v_cvt_pk_bf16_f32 v101, v102, v103\n"
  "v_bfi_b32 v116, v44, 0, v100\n"
  "v_bfi_b32 v117, v45, 0, v101\n"
  "v_and_b32 v118, v44, v100\n"
  "v_and_b32 v119, v45, v101\n"
  "s_waitcnt lgkmcnt(5)\n"
  "v_pk_mul_f32 v[104:105], v[104:105], v[252:253] op_sel_hi:[1,0]\n"
  "v_pk_mul_f32 v[106:107], v[106:107], v[252:253] op_sel_hi:[1,0]\n"
  "v_cvt_pk_bf16_f32 v104, v104, v105\n"
  "v_cvt_pk_bf16_f32 v105, v106, v107\n"
  "v_bfi_b32 v120, v44, 0, v104\n"
  "v_bfi_b32 v121, v45, 0, v105\n"
  "v_and_b32 v122, v44, v104\n"
  "v_and_b32 v123, v45, v105\n"
  "v_mfma_f32_16x16x32_bf16 v[84:87], v[234:237], v[116:119], 0\n"
  "v_mfma_f32_16x16x32_bf16 v[68:71], v[224:227], v[116:119], 0\n"
  "s_waitcnt lgkmcnt(0)\n"
  "v_pk_mul_f32 v[108:109], v[108:109], v[2:3] op_sel_hi:[1,0]\n"
  "v_pk_mul_f32 v[110:111], v[110:111], v[2:3] op_sel_hi:[1,0]\n"
  "v_cvt_pk_bf16_f32 v108, v108, v109\n"
  "v_cvt_pk_bf16_f32 v109, v110, v111\n"
  "v_bfi_b32 v124, v44, 0, v108\n"
  "v_bfi_b32 v125, v45, 0, v109\n"
  "v_and_b32 v126, v44, v108\n"
  "v_and_b32 v127, v45, v109\n"
  "v_mfma_f32_16x16x32_bf16 v[84:87], v[238:241], v[120:123], v[84:87]\n"
  "v_mfma_f32_16x16x32_bf16 v[72:75], v[226:229], v[120:123], 0\n"
  "v_pk_mul_f32 v[112:113], v[112:113], v[4:5] op_sel_hi:[1,0]\n"
  "v_pk_mul_f32 v[114:115], v[114:115], v[4:5] op_sel_hi:[1,0]\n"
  "v_cvt_pk_bf16_f32 v112, v112, v113\n"
  "v_cvt_pk_bf16_f32 v113, v114, v115\n"
  "v_bfi_b32 v128, v44, 0, v112\n"
  "v_bfi_b32 v129, v45, 0, v113\n"
  "v_and_b32 v130, v44, v112\n"
  "v_and_b32 v131, v45, v113\n"
  "v_mfma_f32_16x16x32_bf16 v[84:87], v[242:245], v[124:127], v[84:87]\n"
  "v_mfma_f32_16x16x32_bf16 v[76:79], v[228:231], v[124:127], 0\n"
  "v_mfma_f32_16x16x32_bf16 v[84:87], v[246:249], v[128:131], v[84:87]\n"
  "v_mfma_f32_16x16x32_bf16 v[80:83], v[230:233], v[128:131], 0\n"
  "v_cvt_pk_bf16_f32 v68, v68, v69\n"
  "v_cvt_pk_bf16_f32 v69, v70, v71\n"
  "s_nop 3\n"
  "v_cvt_pk_bf16_f32 v70, v76, v77\n"
  "v_cvt_pk_bf16_f32 v71, v78, v79\n"
  "s_nop 0\n"
  "v_permlane32_swap_b32 v68, v70\n"
  "v_permlane32_swap_b32 v69, v71\n"
  "v_cvt_pk_bf16_f32 v84, v84, v85\n"
  "v_cvt_pk_bf16_f32 v85, v86, v87\n"
  "s_cmp_lg_u32 s24, 0\n"
  "s_cbranch_scc1 .Lnc\n"
  "global_load_dwordx2 v[10:11], v9, s[20:21] sc1\n"
  ".Lnc:\n"
  "global_store_dwordx2 v61, v[84:85], s[20:21] sc1\n"
  "global_store_dwordx4 v60, v[68:71], s[20:21] sc1\n"
  "v_cvt_pk_bf16_f32 v72, v72, v73\n"
  "v_cvt_pk_bf16_f32 v73, v74, v75\n"
  "v_cvt_pk_bf16_f32 v74, v80, v81\n"
  "v_cvt_pk_bf16_f32 v75, v82, v83\n"
  "s_nop 0\n"
  "v_permlane32_swap_b32 v72, v74\n"
  "v_permlane32_swap_b32 v73, v75\n"
  "global_store_dwordx4 v60, v[72:75], s[60:61] sc1\n"
  "s_add_u32 m0, s46, 3072\n"
  "s_nop 0\n"
  "global_load_lds_dwordx4 v17, s[56:57]\n"
  "ds_read_b64_tr_b16 v[160:161], v8 offset:5152\n"
  "ds_read_b64_tr_b16 v[162:163], v8 offset:6432\n"
  "ds_read_b64_tr_b16 v[164:165], v8 offset:5184\n"
  "ds_read_b64_tr_b16 v[166:167], v8 offset:6464\n"
  "ds_read_b64_tr_b16 v[168:169], v8 offset:5216\n"
  "ds_read_b64_tr_b16 v[170:171], v8 offset:6496\n"
  "ds_read_b64_tr_b16 v[172:173], v8 offset:5248\n"
  "ds_read_b64_tr_b16 v[174:175], v8 offset:6528\n"
  "ds_read_b64_tr_b16 v[176:177], v8 offset:10240\n"
  "ds_read_b64_tr_b16 v[178:179], v8 offset:11520\n"
  "s_add_u32 m0, s46, 4096\n"
  "s_nop 0\n"
  "global_load_lds_dwordx4 v18, s[56:57]\n"
  "ds_read_b64_tr_b16 v[180:181], v8 offset:10272\n"
  "ds_read_b64_tr_b16 v[182:183], v8 offset:11552\n"
  "ds_read_b64_tr_b16 v[184:185], v8 offset:10304\n"
  "ds_read_b64_tr_b16 v[186:187], v8 offset:11584\n"
  "ds_read_b64_tr_b16 v[188:189], v8 offset:10336\n"
  "s_waitcnt lgkmcnt(11)\n"
  "ds_read_b64_tr_b16 v[190:191], v8 offset:11616\n"
  "ds_read_b64_tr_b16 v[192:193], v8 offset:10368\n"
  "ds_read_b64_tr_b16 v[194:195], v8 offset:11648\n"
  "ds_read_b64_tr_b16 v[196:197], v8 offset:15360\n"
  "s_waitcnt lgkmcnt(11)\n"
  "ds_read_b64_tr_b16 v[198:199], v8 offset:16640\n"
  "ds_read_b64_tr_b16 v[200:201], v8 offset:15392\n"
  "ds_read_b64_tr_b16 v[202:203], v8 offset:16672\n"
  "ds_read_b64_tr_b16 v[204:205], v8 offset:15424\n"
  "s_waitcnt lgkmcnt(11)\n"
  "ds_read_b64_tr_b16 v[206:207], v8 offset:16704\n"
  "ds_read_b64_tr_b16 v[208:209], v8 offset:15456\n"
  "ds_read_b64_tr_b16 v[210:211], v8 offset:16736\n"
  "ds_read_b64_tr_b16 v[212:213], v8 offset:15488\n"
  "s_waitcnt lgkmcnt(11)\n"
  "ds_read_b64_tr_b16 v[214:215], v8 offset:16768\n"
  "s_add_u32 s32, s31, s47\n"
  "s_add_u32 s32, s32, 8\n"
  "s_and_b32 s32, s32, 15\n"
  "s_mul_i32 s32, s32, 51200\n"
  "s_add_u32 s56, s54, s32\n"
  "s_addc_u32 s57, s55, 0\n"
  "s_waitcnt vmcnt(5) lgkmcnt(0)\n"
  "s_cmp_lg_u32 s24, 0\n"
  "s_cbranch_scc1 .Lpolln\n"
  "v_cmp_ne_u64 vcc, s[62:63], v[10:11]\n"
  "s_mov_b32 s42, 0\n"
  "s_cmp_eq_u64 vcc, 0\n"
  "s_cbranch_scc0 .Lpoll\n"
  ".Lpolln:\n"
  "s_barrier\n"
  "v_add_u32 v130, 102400, v62\n"
  "s_setprio 3\n"
  "s_add_u32 m0, s46, 40960\n"
  "s_nop 0\n"
  "global_load_lds_dwordx4 v14, s[56:57]\n"
  "s_mul_i32 s38, s30, 2\n"
  "s_xor_b32 s38, s38, s31\n"
  "s_and_b32 s6, s27, 1\n"
  "s_lshl_b32 s6, s6, 1\n"
  "s_lshr_b32 s7, s38, 1\n"
  "s_add_u32 s6, s6, s7\n"
  "s_and_b32 s7, s38, 1\n"
  "s_lshl_b32 s45, s7, 2\n"
  "s_add_u32 s45, s45, s6\n"
  "s_lshl_b32 s6, s6, 5\n"
  "s_add_u32 s6, s6, s7\n"
  "s_lshl_b32 s7, s44, 7\n"
  "ds_read_b64_tr_b16 v[88:89], v130\n"
  "ds_read_b64_tr_b16 v[90:91], v130 offset:1280\n"
  "ds_read_b64_tr_b16 v[92:93], v130 offset:32\n"
  "ds_read_b64_tr_b16 v[94:95], v130 offset:1312\n"
  "s_add_u32 m0, s46, 41984\n"
  "s_nop 0\n"
  "global_load_lds_dwordx4 v15, s[56:57]\n"
  "s_add_u32 s6, s6, s7\n"
  "s_mul_i32 s6, s6, 1280\n"
  "s_mul_i32 s49, s43, 160\n"
  "s_add_u32 s6, s6, s49\n"
  "v_mul_u32_u24 v2, 2560, v7\n"
  "v_add_u32 v63, s6, v2\n"
  "v_and_b32 v2, 1, v6\n"
  "v_lshlrev_b32 v2, 5, v2\n"
  "v_lshrrev_b32 v3, 1, v6\n"
  "v_lshl_add_u32 v2, v3, 4, v2\n"
  "v_add_u32 v12, v63, v2\n"
  "v_lshl_add_u32 v63, v6, 3, v63\n"
  "ds_read_b64_tr_b16 v[96:97], v130 offset:64\n"
  "ds_read_b64_tr_b16 v[98:99], v130 offset:1344\n"
  "ds_read_b64_tr_b16 v[100:101], v130 offset:96\n"
  "ds_read_b64_tr_b16 v[102:103], v130 offset:1376\n"
  "s_add_u32 m0, s46, 43008\n"
  "s_nop 0\n"
  "global_load_lds_dwordx4 v16, s[56:57]\n"
  "s_lshl_b32 s6, s44, 3\n"
  "s_add_u32 s6, s6, s45\n"
  "s_lshl_b32 s7, s6, 15\n"
  "s_lshl_b32 s49, s30, 14\n"
  "s_add_u32 s7, s7, s49\n"
  "s_add_u32 s8, s20, s7\n"
  "s_addc_u32 s9, s21, 0\n"
  "s_lshr_b32 s7, s7, 2\n"
  "s_add_u32 s7, s7, 4194304\n"
  "s_add_u32 s52, s20, s7\n"
  "s_addc_u32 s53, s21, 0\n"
  "s_lshl_b32 s7, s6, 6\n"
  "ds_read_b64_tr_b16 v[104:105], v130 offset:128\n"
  "ds_read_b64_tr_b16 v[106:107], v130 offset:1408\n"
  "ds_read_b64_tr_b16 v[108:109], v130 offset:5120\n"
  "ds_read_b64_tr_b16 v[110:111], v130 offset:6400\n"
  "s_add_u32 m0, s46, 44032\n"
  "s_nop 0\n"
  "global_load_lds_dwordx4 v17, s[56:57]\n"
  "s_add_u32 s7, s7, 5242880\n"
  "v_lshl_add_u32 v128, v7, 2, s7\n"
  "v_lshlrev_b32 v129, 3, v1\n"
  "s_mul_i32 s6, s30, 40960\n"
  "s_add_u32 s40, s46, s6\n"
  "s_add_u32 s6, s6, 81920\n"
  "v_add_u32 v132, s6, v62\n"
  "v_add_u32 v131, 40960, v62\n"
  "s_mul_i32 s6, s38, 5120\n"
  "s_add_u32 s6, s6, 102400\n"
  "v_add_u32 v133, s6, v66\n"
  "ds_read_b64_tr_b16 v[112:113], v130 offset:5152\n"
  "ds_read_b64_tr_b16 v[114:115], v130 offset:6432\n"
  "ds_read_b64_tr_b16 v[116:117], v130 offset:5184\n"
  "s_waitcnt lgkmcnt(11)\n"
  "ds_read_b64_tr_b16 v[118:119], v130 offset:6464\n"
  "s_add_u32 m0, s46, 45056\n"
  "s_nop 0\n"
  "global_load_lds_dwordx4 v18, s[56:57]\n"
  "ds_read_b64_tr_b16 v[120:121], v130 offset:5216\n"
  "ds_read_b64_tr_b16 v[122:123], v130 offset:6496\n"
  "ds_read_b64_tr_b16 v[124:125], v130 offset:5248\n"
  "s_waitcnt lgkmcnt(11)\n"
  "ds_read_b64_tr_b16 v[126:127], v130 offset:6528\n"
  "s_setprio 2\n"
  "s_add_u32 s32, s31, s47\n"
  "s_add_u32 s32, s32, 12\n"
  "s_and_b32 s32, s32, 15\n"
  "s_mul_i32 s32, s32, 51200\n"
  "s_add_u32 s56, s54, s32\n"
  "s_addc_u32 s57, s55, 0\n"
  "s_add_u32 m0, s40, 81920\n"
  "s_nop 0\n"
  "global_load_lds_dwordx4 v14, s[56:57]\n"
  "ds_read_b64_tr_b16 v[216:217], v130 offset:10240\n"
  "ds_read_b64_tr_b16 v[218:219], v130 offset:11520\n"
  "ds_read_b64_tr_b16 v[220:221], v130 offset:10272\n"
  "s_waitcnt lgkmcnt(11)\n"
  "ds_read_b64_tr_b16 v[222:223], v130 offset:11552\n"
  "s_add_u32 m0, s40, 82944\n"
  "s_nop 0\n"
  "global_load_lds_dwordx4 v15, s[56:57]\n"
  "ds_read_b64_tr_b16 v[224:225], v130 offset:10304\n"
  "ds_read_b64_tr_b16 v[226:227], v130 offset:11584\n"
  "ds_read_b64_tr_b16 v[228:229], v130 offset:10336\n"
  "s_waitcnt lgkmcnt(11)\n"
  "ds_read_b64_tr_b16 v[230:231], v130 offset:11616\n"
  "s_setprio 1\n"
  "s_add_u32 s32, s51, 0\n"
  "s_and_b32 s32, s32, 3\n"
  "s_lshl_b32 s32, s32, 10\n"
  "v_add_u32 v5, s32, v129\n"
  "global_load_dwordx2 v[24:25], v5, s[52:53] sc1\n"
  "global_load_dwordx2 v[26:27], v5, s[52:53] offset:512 sc1\n"
  "s_add_u32 s32, s51, 1\n"
  "s_and_b32 s32, s32, 3\n"
  "s_lshl_b32 s32, s32, 10\n"
  "v_add_u32 v5, s32, v129\n"
  "global_load_dwordx2 v[28:29], v5, s[52:53] sc1\n"
  "global_load_dwordx2 v[30:31], v5, s[52:53] offset:512 sc1\n"
  "s_add_u32 s32, s51, 2\n"
  "s_and_b32 s32, s32, 3\n"
  "s_lshl_b32 s32, s32, 10\n"
  "v_add_u32 v5, s32, v129\n"
  "global_load_dwordx2 v[32:33], v5, s[52:53] sc1\n"
  "global_load_dwordx2 v[34:35], v5, s[52:53] offset:512 sc1\n"
  "s_add_u32 s32, s51, 3\n"
  "s_and_b32 s32, s32, 3\n"
  "s_lshl_b32 s32, s32, 10\n"
  "v_add_u32 v5, s32, v129\n"
  "global_load_dwordx2 v[36:37], v5, s[52:53] sc1\n"
  "global_load_dwordx2 v[38:39], v5, s[52:53] offset:512 sc1\n"
  "s_setprio 0\n"
  "s_add_u32 m0, s40, 83968\n"
  "s_nop 0\n"
  "global_load_lds_dwordx4 v16, s[56:57]\n"
  "ds_read_b64_tr_b16 v[232:233], v130 offset:10368\n"
  "ds_read_b64_tr_b16 v[234:235], v130 offset:11648\n"
  "ds_read_b64_tr_b16 v[236:237], v130 offset:15360\n"
  "s_waitcnt lgkmcnt(11)\n"
  "ds_read_b64_tr_b16 v[238:239], v130 offset:16640\n"
  "s_add_u32 m0, s40, 84992\n"
  "s_nop 0\n"
  "global_load_lds_dwordx4 v17, s[56:57]\n"
  "ds_read_b64_tr_b16 v[240:241], v130 offset:15392\n"
  "ds_read_b64_tr_b16 v[242:243], v130 offset:16672\n"
  "ds_read_b64_tr_b16 v[244:245], v130 offset:15424\n"
  "s_waitcnt lgkmcnt(11)\n"
  "ds_read_b64_tr_b16 v[246:247], v130 offset:16704\n"
  "s_add_u32 m0, s40, 86016\n"
  "s_nop 0\n"
  "global_load_lds_dwordx4 v18, s[56:57]\n"
  "ds_read_b64_tr_b16 v[248:249], v130 offset:15456\n"
  "ds_read_b64_tr_b16 v[250:251], v130 offset:16736\n"
  "ds_read_b64_tr_b16 v[252:253], v130 offset:15488\n"
  "s_waitcnt lgkmcnt(11)\n"
  "ds_read_b64_tr_b16 v[254:255], v130 offset:16768\n"
  "s_mov_b32 s41, 0\n"
  "s_waitcnt vmcnt(9)\n"
  "v_max3_u32 v2, v24, v25, v26\n"
  "v_max_u32 v2, v2, v27\n"
  "v_cmp_eq_u32 vcc, -1, v2\n"
  "s_nop 0\n"
  "s_cbranch_vccnz .Lsl0\n"
  ".Lok0:\n"
  "v_mfma_f32_16x16x32_bf16 v[68:71], v[136:139], v[24:27], 0\n"
  "v_mfma_f32_16x16x32_bf16 v[72:75], v[140:143], v[24:27], 0\n"
  "v_mfma_f32_16x16x32_bf16 v[76:79], v[144:147], v[24:27], 0\n"
  "s_waitcnt vmcnt(7)\n"
  "v_max3_u32 v2, v28, v29, v30\n"
  "v_max_u32 v2, v2, v31\n"
  "v_cmp_eq_u32 vcc, -1, v2\n"
  "v_mfma_f32_16x16x32_bf16 v[80:83], v[148:151], v[24:27], 0\n"
  "v_mfma_f32_16x16x32_bf16 v[84:87], v[152:155], v[24:27], 0\n"
  "s_add_u32 s32, s47, 0\n"
  "s_and_b32 s32, s32, 15\n"
  "s_lshl_b32 s32, s32, 10\n"
  "v_add_u32 v5, s32, v66\n"
  "global_load_dwordx4 v[40:43], v5, s[8:9] sc1\n"
  "s_cbranch_vccnz .Lsl1\n"
  ".Lok1:\n"
  "v_mfma_f32_16x16x32_bf16 v[68:71], v[156:159], v[28:31], v[68:71]\n"
  "v_mfma_f32_16x16x32_bf16 v[72:75], v[160:163], v[28:31], v[72:75]\n"
  "v_mfma_f32_16x16x32_bf16 v[76:79], v[164:167], v[28:31], v[76:79]\n"
  "s_waitcnt vmcnt(6)\n"
  "v_max3_u32 v2, v32, v33, v34\n"
  "v_max_u32 v2, v2, v35\n"
  "v_cmp_eq_u32 vcc, -1, v2\n"
  "v_mfma_f32_16x16x32_bf16 v[80:83], v[168:171], v[28:31], v[80:83]\n"
  "v_mfma_f32_16x16x32_bf16 v[84:87], v[172:175], v[28:31], v[84:87]\n"
  "global_load_dwordx4 v[44:47], v5, s[8:9] offset:1024 sc1\n"
  "s_cbranch_vccnz .Lsl2\n"
  ".Lok2:\n"
  "v_mfma_f32_16x16x32_bf16 v[68:71], v[176:179], v[32:35], v[68:71]\n"
  "v_mfma_f32_16x16x32_bf16 v[72:75], v[180:183], v[32:35], v[72:75]\n"
  "v_mfma_f32_16x16x32_bf16 v[76:79], v[184:187], v[32:35], v[76:79]\n"
  "s_waitcnt vmcnt(5)\n"
  "v_max3_u32 v2, v36, v37, v38\n"
  "v_max_u32 v2, v2, v39\n"
  "v_cmp_eq_u32 vcc, -1, v2\n"
  "v_mfma_f32_16x16x32_bf16 v[80:83], v[188:191], v[32:35], v[80:83]\n"
  "v_mfma_f32_16x16x32_bf16 v[84:87], v[192:195], v[32:35], v[84:87]\n"
  "s_add_u32 s32, s47, 2\n"
  "s_and_b32 s32, s32, 15\n"
  "s_lshl_b32 s32, s32, 10\n"
  "v_add_u32 v5, s32, v66\n"
  "global_load_dwordx4 v[48:51], v5, s[8:9] sc1\n"
  "s_cbranch_vccnz .Lsl3\n"
  ".Lok3:\n"
  "v_mfma_f32_16x16x32_bf16 v[68:71], v[196:199], v[36:39], v[68:71]\n"
  "v_mfma_f32_16x16x32_bf16 v[72:75], v[200:203], v[36:39], v[72:75]\n"
  "v_mfma_f32_16x16x32_bf16 v[76:79], v[204:207], v[36:39], v[76:79]\n"
  "s_waitcnt vmcnt(2)\n"
  "v_max3_u32 v2, v40, v41, v42\n"
  "v_max_u32 v2, v2, v43\n"
  "v_cmp_eq_u32 vcc, -1, v2\n"
  "v_mfma_f32_16x16x32_bf16 v[80:83], v[208:211], v[36:39], v[80:83]\n"
  "v_mfma_f32_16x16x32_bf16 v[84:87], v[212:215], v[36:39], v[84:87]\n"
  "global_load_dwordx4 v[52:55], v5, s[8:9] offset:1024 sc1\n"
  "s_cbranch_vccnz .Lsl4\n"
  ".Lok4:\n"
  "s_waitcnt lgkmcnt(0)\n"
  "s_barrier\n"
  "s_setprio 3\n"
  "v_mfma_f32_16x16x32_bf16 v[68:71], v[88:91], v[40:43], v[68:71]\n"
  "ds_read_b64_tr_b16 v[136:137], v62\n"
  "v_mfma_f32_16x16x32_bf16 v[72:75], v[92:95], v[40:43], v[72:75]\n"
  "ds_read_b64_tr_b16 v[138:139], v62 offset:1280\n"
  "v_mfma_f32_16x16x32_bf16 v[76:79], v[96:99], v[40:43], v[76:79]\n"
  "ds_read_b64_tr_b16 v[140:141], v62 offset:32\n"
  "ds_read_b64_tr_b16 v[142:143], v62 offset:1312\n"
  "s_waitcnt vmcnt(2)\n"
  "v_max3_u32 v2, v44, v45, v46\n"
  "v_max_u32 v2, v2, v47\n"
  "v_cmp_eq_u32 vcc, -1, v2\n"
  "v_mfma_f32_16x16x32_bf16 v[80:83], v[100:103], v[40:43], v[80:83]\n"
  "ds_read_b64_tr_b16 v[144:145], v62 offset:64\n"
  "ds_read_b64_tr_b16 v[146:147], v62 offset:1344\n"
  "v_mfma_f32_16x16x32_bf16 v[84:87], v[104:107], v[40:43], v[84:87]\n"
  "ds_read_b64_tr_b16 v[148:149], v62 offset:96\n"
  "ds_read_b64_tr_b16 v[150:151], v62 offset:1376\n"
  "s_add_u32 s32, s47, 4\n"
  "s_and_b32 s32, s32, 15\n"
  "s_lshl_b32 s32, s32, 10\n"
  "v_add_u32 v5, s32, v66\n"
  "global_load_dwordx4 v[56:59], v5, s[8:9] sc1\n"
  "s_cbranch_vccnz .Lsl5\n"
  ".Lok5:\n"
  "v_mfma_f32_16x16x32_bf16 v[68:71], v[108:111], v[44:47], v[68:71]\n"
  "ds_read_b64_tr_b16 v[152:153], v62 offset:128\n"
  "v_mfma_f32_16x16x32_bf16 v[72:75], v[112:115], v[44:47], v[72:75]\n"
  "ds_read_b64_tr_b16 v[154:155], v62 offset:1408\n"
  "ds_read_b64_tr_b16 v[156:157], v62 offset:5120\n"
  "global_load_dwordx4 v[8:11], v5, s[8:9] offset:1024 sc1\n"
  "v_mfma_f32_16x16x32_bf16 v[76:79], v[116:119], v[44:47], v[76:79]\n"
  "ds_read_b64_tr_b16 v[158:159], v62 offset:6400\n"
  "ds_read_b64_tr_b16 v[160:161], v62 offset:5152\n"
  "s_waitcnt vmcnt(3)\n"
  "v_max3_u32 v2, v48, v49, v50\n"
  "v_max_u32 v2, v2, v51\n"
  "v_cmp_eq_u32 vcc, -1, v2\n"
  "v_mfma_f32_16x16x32_bf16 v[80:83], v[120:123], v[44:47], v[80:83]\n"
  "ds_read_b64_tr_b16 v[162:163], v62 offset:6432\n"
  "ds_read_b64_tr_b16 v[164:165], v62 offset:5184\n"
  "v_mfma_f32_16x16x32_bf16 v[84:87], v[124:127], v[44:47], v[84:87]\n"
  "s_waitcnt lgkmcnt(11)\n"
  "ds_read_b64_tr_b16 v[166:167], v62 offset:6464\n"
  "s_add_u32 s32, s47, 6\n"
  "s_and_b32 s32, s32, 15\n"
  "s_lshl_b32 s32, s32, 10\n"
  "v_add_u32 v5, s32, v66\n"
  "global_load_dwordx4 v[24:27], v5, s[8:9] sc1\n"
  "s_cbranch_vccnz .Lsl6\n"
  ".Lok6:\n"
  "s_setprio 2\n"
  "v_mfma_f32_16x16x32_bf16 v[68:71], v[216:219], v[48:51], v[68:71]\n"
  "ds_read_b64_tr_b16 v[168:169], v62 offset:5216\n"
  "ds_read_b64_tr_b16 v[170:171], v62 offset:6496\n"
  "v_mfma_f32_16x16x32_bf16 v[72:75], v[220:223], v[48:51], v[72:75]\n"
  "ds_read_b64_tr_b16 v[172:173], v62 offset:5248\n"
  "s_waitcnt lgkmcnt(11)\n"
  "ds_read_b64_tr_b16 v[174:175], v62 offset:6528\n"
  "v_mfma_f32_16x16x32_bf16 v[76:79], v[224:227], v[48:51], v[76:79]\n"
  "ds_read_b64_tr_b16 v[176:177], v62 offset:10240\n"
  "ds_read_b64_tr_b16 v[178:179], v62 offset:11520\n"
  "s_waitcnt vmcnt(3)\n"
  "v_max3_u32 v2, v52, v53, v54\n"
  "v_max_u32 v2, v2, v55\n"
  "v_cmp_eq_u32 vcc, -1, v2\n"
  "v_mfma_f32_16x16x32_bf16 v[80:83], v[228:231], v[48:51], v[80:83]\n"
  "ds_read_b64_tr_b16 v[180:181], v62 offset:10272\n"
  "v_mfma_f32_16x16x32_bf16 v[84:87], v[232:235], v[48:51], v[84:87]\n"
  "s_waitcnt lgkmcnt(11)\n"
  "ds_read_b64_tr_b16 v[182:183], v62 offset:11552\n"
  "ds_read_b64_tr_b16 v[184:185], v62 offset:10304\n"
  "global_load_dwordx4 v[28:31], v5, s[8:9] offset:1024 sc1\n"
  "s_cbranch_vccnz .Lsl7\n"
  ".Lok7:\n"
  "v_mfma_f32_16x16x32_bf16 v[68:71], v[236:239], v[52:55], v[68:71]\n"
  "ds_read_b64_tr_b16 v[186:187], v62 offset:11584\n"
  "ds_read_b64_tr_b16 v[188:189], v62 offset:10336\n"
  "v_mfma_f32_16x16x32_bf16 v[72:75], v[240:243], v[52:55], v[72:75]\n"
  "s_waitcnt lgkmcnt(11)\n"
  "ds_read_b64_tr_b16 v[190:191], v62 offset:11616\n"
  "ds_read_b64_tr_b16 v[192:193], v62 offset:10368\n"
  "s_add_u32 s32, s47, 8\n"
  "s_and_b32 s32, s32, 15\n"
  "s_lshl_b32 s32, s32, 10\n"
  "v_add_u32 v5, s32, v66\n"
  "global_load_dwordx4 v[32:35], v5, s[8:9] sc1\n"
  "v_mfma_f32_16x16x32_bf16 v[76:79], v[244:247], v[52:55], v[76:79]\n"
  "ds_read_b64_tr_b16 v[194:195], v62 offset:11648\n"
  "ds_read_b64_tr_b16 v[196:197], v62 offset:15360\n"
  "s_waitcnt vmcnt(4)\n"
  "v_max3_u32 v2, v56, v57, v58\n"
  "v_max_u32 v2, v2, v59\n"
  "v_cmp_eq_u32 vcc, -1, v2\n"
  "v_mfma_f32_16x16x32_bf16 v[80:83], v[248:251], v[52:55], v[80:83]\n"
  "s_waitcnt lgkmcnt(11)\n"
  "ds_read_b64_tr_b16 v[198:199], v62 offset:16640\n"
  "v_mfma_f32_16x16x32_bf16 v[84:87], v[252:255], v[52:55], v[84:87]\n"
  "ds_read_b64_tr_b16 v[200:201], v62 offset:15392\n"
  "ds_read_b64_tr_b16 v[202:203], v62 offset:16672\n"
  "global_load_dwordx4 v[36:39], v5, s[8:9] offset:1024 sc1\n"
  "s_cbranch_vccnz .Lsl8\n"
  ".Lok8:\n"
  "s_setprio 1\n"
  "v_mfma_f32_16x16x32_bf16 v[68:71], v[136:139], v[56:59], v[68:71]\n"
  "ds_read_b64_tr_b16 v[204:205], v62 offset:15424\n"
  "s_waitcnt lgkmcnt(11)\n"
  "ds_read_b64_tr_b16 v[206:207], v62 offset:16704\n"
  "v_mfma_f32_16x16x32_bf16 v[72:75], v[140:143], v[56:59], v[72:75]\n"
  "ds_read_b64_tr_b16 v[208:209], v62 offset:15456\n"
  "ds_read_b64_tr_b16 v[210:211], v62 offset:16736\n"
  "v_mfma_f32_16x16x32_bf16 v[76:79], v[144:147], v[56:59], v[76:79]\n"
  "ds_read_b64_tr_b16 v[212:213], v62 offset:15488\n"
  "s_waitcnt vmcnt(4)\n"
  "v_max3_u32 v2, v8, v9, v10\n"
  "v_max_u32 v2, v2, v11\n"
  "v_cmp_eq_u32 vcc, -1, v2\n"
  "v_mfma_f32_16x16x32_bf16 v[80:83], v[148:151], v[56:59], v[80:83]\n"
  "s_waitcnt lgkmcnt(11)\n"
  "ds_read_b64_tr_b16 v[214:215], v62 offset:16768\n"
  "ds_read_b64_tr_b16 v[88:89], v131\n"
  "v_mfma_f32_16x16x32_bf16 v[84:87], v[152:155], v[56:59], v[84:87]\n"
  "ds_read_b64_tr_b16 v[90:91], v131 offset:1280\n"
  "ds_read_b64_tr_b16 v[92:93], v131 offset:32\n"
  "s_add_u32 s32, s47, 10\n"
  "s_and_b32 s32, s32, 15\n"
  "s_lshl_b32 s32, s32, 10\n"
  "v_add_u32 v5, s32, v66\n"
  "global_load_dwordx4 v[40:43], v5, s[8:9] sc1\n"
  "s_cbranch_vccnz .Lsl9\n"
  ".Lok9:\n"
  "v_mfma_f32_16x16x32_bf16 v[68:71], v[156:159], v[8:11], v[68:71]\n"
  "s_waitcnt lgkmcnt(11)\n"
  "ds_read_b64_tr_b16 v[94:95], v131 offset:1312\n"
  "ds_read_b64_tr_b16 v[96:97], v131 offset:64\n"
  "v_mfma_f32_16x16x32_bf16 v[72:75], v[160:163], v[8:11], v[72:75]\n"
  "ds_read_b64_tr_b16 v[98:99], v131 offset:1344\n"
  "v_mfma_f32_16x16x32_bf16 v[76:79], v[164:167], v[8:11], v[76:79]\n"
  "ds_read_b64_tr_b16 v[100:101], v131 offset:96\n"
  "s_waitcnt lgkmcnt(11)\n"
  "ds_read_b64_tr_b16 v[102:103], v131 offset:1376\n"
  "s_waitcnt vmcnt(4)\n"
  "v_max3_u32 v2, v24, v25, v26\n"
  "v_max_u32 v2, v2, v27\n"
  "v_cmp_eq_u32 vcc, -1, v2\n"
  "v_mfma_f32_16x16x32_bf16 v[80:83], v[168:171], v[8:11], v[80:83]\n"
  "ds_read_b64_tr_b16 v[104:105], v131 offset:128\n"
  "ds_read_b64_tr_b16 v[106:107], v131 offset:1408\n"
  "v_mfma_f32_16x16x32_bf16 v[84:87], v[172:175], v[8:11], v[84:87]\n"
  "ds_read_b64_tr_b16 v[108:109], v131 offset:5120\n"
  "s_waitcnt lgkmcnt(11)\n"
  "ds_read_b64_tr_b16 v[110:111], v131 offset:6400\n"
  "global_load_dwordx4 v[44:47], v5, s[8:9] offset:1024 sc1\n"
  "s_cbranch_vccnz .Lsl10\n"
  ".Lok10:\n"
  "s_setprio 0\n"
  "v_mfma_f32_16x16x32_bf16 v[68:71], v[176:179], v[24:27], v[68:71]\n"
  "ds_read_b64_tr_b16 v[112:113], v131 offset:5152\n"
  "v_mfma_f32_16x16x32_bf16 v[72:75], v[180:183], v[24:27], v[72:75]\n"
  "ds_read_b64_tr_b16 v[114:115], v131 offset:6432\n"
  "ds_read_b64_tr_b16 v[116:117], v131 offset:5184\n"
  "v_mfma_f32_16x16x32_bf16 v[76:79], v[184:187], v[24:27], v[76:79]\n"
  "s_waitcnt lgkmcnt(11)\n"
  "ds_read_b64_tr_b16 v[118:119], v131 offset:6464\n"
  "ds_read_b64_tr_b16 v[120:121], v131 offset:5216\n"
  "s_waitcnt vmcnt(4)\n"
  "v_max3_u32 v2, v28, v29, v30\n"
  "v_max_u32 v2, v2, v31\n"
  "v_cmp_eq_u32 vcc, -1, v2\n"
  "v_mfma_f32_16x16x32_bf16 v[80:83], v[188:191], v[24:27], v[80:83]\n"
  "ds_read_b64_tr_b16 v[122:123], v131 offset:6496\n"
  "ds_read_b64_tr_b16 v[124:125], v131 offset:5248\n"
  "v_mfma_f32_16x16x32_bf16 v[84:87], v[192:195], v[24:27], v[84:87]\n"
  "s_waitcnt lgkmcnt(11)\n"
  "ds_read_b64_tr_b16 v[126:127], v131 offset:6528\n"
  "ds_read_b64_tr_b16 v[216:217], v131 offset:10240\n"
  "s_add_u32 s32, s47, 12\n"
  "s_and_b32 s32, s32, 15\n"
  "s_lshl_b32 s32, s32, 10\n"
  "v_add_u32 v5, s32, v66\n"
  "global_load_dwordx4 v[48:51], v5, s[8:9] sc1\n"
  "s_cbranch_vccnz .Lsl11\n"
  ".Lok11:\n"
  "v_mfma_f32_16x16x32_bf16 v[68:71], v[196:199], v[28:31], v[68:71]\n"
  "ds_read_b64_tr_b16 v[218:219], v131 offset:11520\n"
  "v_mfma_f32_16x16x32_bf16 v[72:75], v[200:203], v[28:31], v[72:75]\n"
  "ds_read_b64_tr_b16 v[220:221], v131 offset:10272\n"
  "s_waitcnt lgkmcnt(11)\n"
  "ds_read_b64_tr_b16 v[222:223], v131 offset:11552\n"
  "v_mfma_f32_16x16x32_bf16 v[76:79], v[204:207], v[28:31], v[76:79]\n"
  "ds_read_b64_tr_b16 v[224:225], v131 offset:10304\n"
  "ds_read_b64_tr_b16 v[226:227], v131 offset:11584\n"
  "s_waitcnt vmcnt(4)\n"
  "v_max3_u32 v2, v32, v33, v34\n"
  "v_max_u32 v2, v2, v35\n"
  "v_cmp_eq_u32 vcc, -1, v2\n"
  "v_mfma_f32_16x16x32_bf16 v[80:83], v[208:211], v[28:31], v[80:83]\n"
  "ds_read_b64_tr_b16 v[228:229], v131 offset:10336\n"
  "s_waitcnt lgkmcnt(11)\n"
  "ds_read_b64_tr_b16 v[230:231], v131 offset:11616\n"
  "v_mfma_f32_16x16x32_bf16 v[84:87], v[212:215], v[28:31], v[84:87]\n"
  "ds_read_b64_tr_b16 v[232:233], v131 offset:10368\n"
  "global_load_dwordx4 v[52:55], v5, s[8:9] offset:1024 sc1\n"
  "s_cbranch_vccnz .Lsl12\n"
  ".Lok12:\n"
  "s_barrier\n"
  "s_setprio 3\n"
  "v_mfma_f32_16x16x32_bf16 v[68:71], v[88:91], v[32:35], v[68:71]\n"
  "ds_read_b64_tr_b16 v[234:235], v131 offset:11648\n"
  "ds_read_b64_tr_b16 v[236:237], v131 offset:15360\n"
  "v_mfma_f32_16x16x32_bf16 v[72:75], v[92:95], v[32:35], v[72:75]\n"
  "s_waitcnt lgkmcnt(11)\n"
  "ds_read_b64_tr_b16 v[238:239], v131 offset:16640\n"
  "ds_read_b64_tr_b16 v[240:241], v131 offset:15392\n"
  "v_mfma_f32_16x16x32_bf16 v[76:79], v[96:99], v[32:35], v[76:79]\n"
  "ds_read_b64_tr_b16 v[242:243], v131 offset:16672\n"
  "ds_read_b64_tr_b16 v[244:245], v131 offset:15424\n"
  "s_waitcnt vmcnt(4)\n"
  "v_max3_u32 v2, v36, v37, v38\n"
  "v_max_u32 v2, v2, v39\n"
  "v_cmp_eq_u32 vcc, -1, v2\n"
  "v_mfma_f32_16x16x32_bf16 v[80:83], v[100:103], v[32:35], v[80:83]\n"
  "s_waitcnt lgkmcnt(11)\n"
  "ds_read_b64_tr_b16 v[246:247], v131 offset:16704\n"
  "v_mfma_f32_16x16x32_bf16 v[84:87], v[104:107], v[32:35], v[84:87]\n"
  "ds_read_b64_tr_b16 v[248:249], v131 offset:15456\n"
  "ds_read_b64_tr_b16 v[250:251], v131 offset:16736\n"
  "s_add_u32 s32, s47, 14\n"
  "s_and_b32 s32, s32, 15\n"
  "s_lshl_b32 s32, s32, 10\n"
  "v_add_u32 v5, s32, v66\n"
  "global_load_dwordx4 v[56:59], v5, s[8:9] sc1\n"
  "s_cbranch_vccnz .Lsl13\n"
  ".Lok13:\n"
  "v_mfma_f32_16x16x32_bf16 v[68:71], v[108:111], v[36:39], v[68:71]\n"
  "ds_read_b64_tr_b16 v[252:253], v131 offset:15488\n"
  "s_waitcnt lgkmcnt(11)\n"
  "ds_read_b64_tr_b16 v[254:255], v131 offset:16768\n"
  "v_mfma_f32_16x16x32_bf16 v[72:75], v[112:115], v[36:39], v[72:75]\n"
  "ds_read_b64_tr_b16 v[136:137], v132\n"
  "ds_read_b64_tr_b16 v[138:139], v132 offset:1280\n"
  "v_mfma_f32_16x16x32_bf16 v[76:79], v[116:119], v[36:39], v[76:79]\n"
  "ds_read_b64_tr_b16 v[140:141], v132 offset:32\n"
  "s_waitcnt vmcnt(4)\n"
  "v_max3_u32 v2, v40, v41, v42\n"
  "v_max_u32 v2, v2, v43\n"
  "v_cmp_eq_u32 vcc, -1, v2\n"
  "v_mfma_f32_16x16x32_bf16 v[80:83], v[120:123], v[36:39], v[80:83]\n"
  "s_waitcnt lgkmcnt(11)\n"
  "ds_read_b64_tr_b16 v[142:143], v132 offset:1312\n"
  "ds_read_b64_tr_b16 v[144:145], v132 offset:64\n"
  "v_mfma_f32_16x16x32_bf16 v[84:87], v[124:127], v[36:39], v[84:87]\n"
  "ds_read_b64_tr_b16 v[146:147], v132 offset:1344\n"
  "ds_read_b64_tr_b16 v[148:149], v132 offset:96\n"
  "global_load_dwordx4 v[8:11], v5, s[8:9] offset:1024 sc1\n"
  "global_load_dword v134, v128, s[20:21] sc1\n"
  "s_cbranch_vccnz .Lsl14\n"
  ".Lok14:\n"
  "s_setprio 2\n"
  "v_mfma_f32_16x16x32_bf16 v[68:71], v[216:219], v[40:43], v[68:71]\n"
  "s_waitcnt lgkmcnt(11)\n"
  "ds_read_b64_tr_b16 v[150:151], v132 offset:1376\n"
  "ds_read_b64_tr_b16 v[152:153], v132 offset:128\n"
  "v_mfma_f32_16x16x32_bf16 v[72:75], v[220:223], v[40:43], v[72:75]\n"
  "ds_read_b64_tr_b16 v[154:155], v132 offset:1408\n"
  "ds_read_b64_tr_b16 v[156:157], v132 offset:5120\n"
  "v_mfma_f32_16x16x32_bf16 v[76:79], v[224:227], v[40:43], v[76:79]\n"
  "s_waitcnt lgkmcnt(11)\n"
  "ds_read_b64_tr_b16 v[158:159], v132 offset:6400\n"
  "s_waitcnt vmcnt(5)\n"
  "v_max3_u32 v2, v44, v45, v46\n"
  "v_max_u32 v2, v2, v47\n"
  "v_cmp_eq_u32 vcc, -1, v2\n"
  "v_mfma_f32_16x16x32_bf16 v[80:83], v[228:231], v[40:43], v[80:83]\n"
  "ds_read_b64_tr_b16 v[160:161], v132 offset:5152\n"
  "ds_read_b64_tr_b16 v[162:163], v132 offset:6432\n"
  "v_mfma_f32_16x16x32_bf16 v[84:87], v[232:235], v[40:43], v[84:87]\n"
  "ds_read_b64_tr_b16 v[164:165], v132 offset:5184\n"
  "s_waitcnt lgkmcnt(11)\n"
  "ds_read_b64_tr_b16 v[166:167], v132 offset:6464\n"
  "s_cbranch_vccnz .Lsl15\n"
  ".Lok15:\n"
  "v_mfma_f32_16x16x32_bf16 v[68:71], v[236:239], v[44:47], v[68:71]\n"
  "ds_read_b64_tr_b16 v[168:169], v132 offset:5216\n"
  "ds_read_b64_tr_b16 v[170:171], v132 offset:6496\n"
  "v_mfma_f32_16x16x32_bf16 v[72:75], v[240:243], v[44:47], v[72:75]\n"
  "ds_read_b64_tr_b16 v[172:173], v132 offset:5248\n"
  "v_mfma_f32_16x16x32_bf16 v[76:79], v[244:247], v[44:47], v[76:79]\n"
  "s_waitcnt lgkmcnt(11)\n"
  "ds_read_b64_tr_b16 v[174:175], v132 offset:6528\n"
  "ds_read_b64_tr_b16 v[176:177], v132 offset:10240\n"
  "s_waitcnt vmcnt(4)\n"
  "v_max3_u32 v2, v48, v49, v50\n"
  "v_max_u32 v2, v2, v51\n"
  "v_cmp_eq_u32 vcc, -1, v2\n"
  "v_mfma_f32_16x16x32_bf16 v[80:83], v[248:251], v[44:47], v[80:83]\n"
  "ds_read_b64_tr_b16 v[178:179], v132 offset:11520\n"
  "ds_read_b64_tr_b16 v[180:181], v132 offset:10272\n"
  "v_mfma_f32_16x16x32_bf16 v[84:87], v[252:255], v[44:47], v[84:87]\n"
  "s_waitcnt lgkmcnt(11)\n"
  "ds_read_b64_tr_b16 v[182:183], v132 offset:11552\n"
  "ds_read_b64_tr_b16 v[184:185], v132 offset:10304\n"
  "s_cbranch_vccnz .Lsl16\n"
  ".Lok16:\n"
  "s_setprio 1\n"
  "v_mfma_f32_16x16x32_bf16 v[68:71], v[136:139], v[48:51], v[68:71]\n"
  "ds_read_b64_tr_b16 v[186:187], v132 offset:11584\n"
  "v_mfma_f32_16x16x32_bf16 v[72:75], v[140:143], v[48:51], v[72:75]\n"
  "ds_read_b64_tr_b16 v[188:189], v132 offset:10336\n"
  "s_waitcnt lgkmcnt(11)\n"
  "ds_read_b64_tr_b16 v[190:191], v132 offset:11616\n"
  "v_mfma_f32_16x16x32_bf16 v[76:79], v[144:147], v[48:51], v[76:79]\n"
  "ds_read_b64_tr_b16 v[192:193], v132 offset:10368\n"
  "ds_read_b64_tr_b16 v[194:195], v132 offset:11648\n"
  "s_waitcnt vmcnt(3)\n"
  "v_max3_u32 v2, v52, v53, v54\n"
  "v_max_u32 v2, v2, v55\n"
  "v_cmp_eq_u32 vcc, -1, v2\n"
  "v_mfma_f32_16x16x32_bf16 v[80:83], v[148:151], v[48:51], v[80:83]\n"
  "ds_read_b64_tr_b16 v[196:197], v132 offset:15360\n"
  "s_waitcnt lgkmcnt(11)\n"
  "ds_read_b64_tr_b16 v[198:199], v132 offset:16640\n"
  "v_mfma_f32_16x16x32_bf16 v[84:87], v[152:155], v[48:51], v[84:87]\n"
  "ds_read_b64_tr_b16 v[200:201], v132 offset:15392\n"
  "s_cbranch_vccnz .Lsl17\n"
  ".Lok17:\n"
  "v_mfma_f32_16x16x32_bf16 v[68:71], v[156:159], v[52:55], v[68:71]\n"
  "ds_read_b64_tr_b16 v[202:203], v132 offset:16672\n"
  "ds_read_b64_tr_b16 v[204:205], v132 offset:15424\n"
  "v_mfma_f32_16x16x32_bf16 v[72:75], v[160:163], v[52:55], v[72:75]\n"
  "s_waitcnt lgkmcnt(11)\n"
  "ds_read_b64_tr_b16 v[206:207], v132 offset:16704\n"
  "ds_read_b64_tr_b16 v[208:209], v132 offset:15456\n"
  "v_mfma_f32_16x16x32_bf16 v[76:79], v[164:167], v[52:55], v[76:79]\n"
  "ds_read_b64_tr_b16 v[210:211], v132 offset:16736\n"
  "ds_read_b64_tr_b16 v[212:213], v132 offset:15488\n"
  "s_waitcnt vmcnt(2)\n"
  "v_max3_u32 v2, v56, v57, v58\n"
  "v_max_u32 v2, v2, v59\n"
  "v_cmp_eq_u32 vcc, -1, v2\n"
  "v_mfma_f32_16x16x32_bf16 v[80:83], v[168:171], v[52:55], v[80:83]\n"
  "s_waitcnt lgkmcnt(11)\n"
  "ds_read_b64_tr_b16 v[214:215], v132 offset:16768\n"
  "v_mfma_f32_16x16x32_bf16 v[84:87], v[172:175], v[52:55], v[84:87]\n"
  "s_cbranch_vccnz .Lsl18\n"
  ".Lok18:\n"
  "s_setprio 0\n"
  "v_mfma_f32_16x16x32_bf16 v[68:71], v[176:179], v[56:59], v[68:71]\n"
  "v_mfma_f32_16x16x32_bf16 v[72:75], v[180:183], v[56:59], v[72:75]\n"
  "v_mfma_f32_16x16x32_bf16 v[76:79], v[184:187], v[56:59], v[76:79]\n"
  "s_waitcnt vmcnt(1)\n"
  "v_max3_u32 v2, v8, v9, v10\n"
  "v_max_u32 v2, v2, v11\n"
  "v_cmp_eq_u32 vcc, -1, v2\n"
  "v_mfma_f32_16x16x32_bf16 v[80:83], v[188:191], v[56:59], v[80:83]\n"
  "s_waitcnt lgkmcnt(0)\n"
  "v_mfma_f32_16x16x32_bf16 v[84:87], v[192:195], v[56:59], v[84:87]\n"
  "s_cbranch_vccnz .Lsl19\n"
  ".Lok19:\n"
  "v_mfma_f32_16x16x32_bf16 v[68:71], v[196:199], v[8:11], v[68:71]\n"
  "v_mfma_f32_16x16x32_bf16 v[72:75], v[200:203], v[8:11], v[72:75]\n"
  "v_mfma_f32_16x16x32_bf16 v[76:79], v[204:207], v[8:11], v[76:79]\n"
  "v_mfma_f32_16x16x32_bf16 v[80:83], v[208:211], v[8:11], v[80:83]\n"
  "v_mfma_f32_16x16x32_bf16 v[84:87], v[212:215], v[8:11], v[84:87]\n"
  "s_waitcnt vmcnt(0)\n"
  "v_cmp_eq_u32 vcc, -1, v134\n"
  "s_nop 0\n"
  "s_cbranch_vccnz .Lslg\n"
  ".Lokg:\n"
  "s_or_b32 s37, s25, s24\n"
  "s_cmp_lg_u32 s37, 0\n"
  "s_cbranch_scc1 .Lnb\n"
  "v_add_u32 v4, 5308416, v129\n"
  "global_load_dwordx2 v[24:25], v4, s[20:21] sc1\n"
  "global_load_dwordx2 v[26:27], v4, s[20:21] offset:512 sc1\n"
  "global_load_dwordx2 v[28:29], v4, s[20:21] offset:1024 sc1\n"
  "global_load_dwordx2 v[30:31], v4, s[20:21] offset:1536 sc1\n"
  ".Lnb:\n"
  "s_cmp_lg_u32 s30, 0\n"
  "s_cbranch_scc0 .LeA\n"
  "ds_write_b128 v133, v[68:71]\n"
  "ds_write_b128 v133, v[72:75] offset:1024\n"
  "ds_write_b128 v133, v[84:87] offset:4096\n"
  "v_sub_f32 v2, 1.0, v134\n"
  "v_pk_mul_f32 v[76:77], v[76:77], v[2:3] op_sel_hi:[1,0]\n"
  "v_pk_mul_f32 v[78:79], v[78:79], v[2:3] op_sel_hi:[1,0]\n"
  "v_pk_mul_f32 v[80:81], v[80:81], v[2:3] op_sel_hi:[1,0]\n"
  "v_pk_mul_f32 v[82:83], v[82:83], v[2:3] op_sel_hi:[1,0]\n"
  "s_waitcnt lgkmcnt(0)\n"
  "s_barrier\n"
  "ds_read_b128 v[88:91], v133 offset:2048\n"
  "ds_read_b128 v[92:95], v133 offset:3072\n"
  "s_waitcnt lgkmcnt(1)\n"
  "v_pk_fma_f32 v[76:77], v[88:89], v[134:135], v[76:77] op_sel_hi:[1,0,1]\n"
  "v_pk_fma_f32 v[78:79], v[90:91], v[134:135], v[78:79] op_sel_hi:[1,0,1]\n"
  "v_cvt_pk_bf16_f32 v76, v76, v77\n"
  "v_cvt_pk_bf16_f32 v77, v78, v79\n"
  "s_waitcnt lgkmcnt(0)\n"
  "v_pk_fma_f32 v[80:81], v[92:93], v[134:135], v[80:81] op_sel_hi:[1,0,1]\n"
  "v_pk_fma_f32 v[82:83], v[94:95], v[134:135], v[82:83] op_sel_hi:[1,0,1]\n"
  "v_cvt_pk_bf16_f32 v78, v80, v81\n"
  "v_cvt_pk_bf16_f32 v79, v82, v83\n"
  "s_nop 0\n"
  "v_permlane16_swap_b32 v76, v78\n"
  "v_permlane16_swap_b32 v77, v79\n"
  "global_store_dwordx4 v12, v[76:79], s[22:23] offset:64\n"
  "s_endpgm\n"
  ".LeA:\n"
  "ds_write_b128 v133, v[76:79] offset:2048\n"
  "ds_write_b128 v133, v[80:83] offset:3072\n"
  "v_sub_f32 v2, 1.0, v134\n"
  "v_pk_mul_f32 v[68:69], v[68:69], v[134:135] op_sel_hi:[1,0]\n"
  "v_pk_mul_f32 v[70:71], v[70:71], v[134:135] op_sel_hi:[1,0]\n"
  "v_pk_mul_f32 v[72:73], v[72:73], v[134:135] op_sel_hi:[1,0]\n"
  "v_pk_mul_f32 v[74:75], v[74:75], v[134:135] op_sel_hi:[1,0]\n"
  "v_pk_mul_f32 v[84:85], v[84:85], v[134:135] op_sel_hi:[1,0]\n"
  "v_pk_mul_f32 v[86:87], v[86:87], v[134:135] op_sel_hi:[1,0]\n"
  "s_waitcnt lgkmcnt(0)\n"
  "s_barrier\n"
  "ds_read_b128 v[88:91], v133\n"
  "ds_read_b128 v[92:95], v133 offset:1024\n"
  "ds_read_b128 v[96:99], v133 offset:4096\n"
  "s_waitcnt lgkmcnt(2)\n"
  "v_pk_fma_f32 v[68:69], v[88:89], v[2:3], v[68:69] op_sel_hi:[1,0,1]\n"
  "v_pk_fma_f32 v[70:71], v[90:91], v[2:3], v[70:71] op_sel_hi:[1,0,1]\n"
  "v_cvt_pk_bf16_f32 v68, v68, v69\n"
  "v_cvt_pk_bf16_f32 v69, v70, v71\n"
  "s_waitcnt lgkmcnt(1)\n"
  "v_pk_fma_f32 v[72:73], v[92:93], v[2:3], v[72:73] op_sel_hi:[1,0,1]\n"
  "v_pk_fma_f32 v[74:75], v[94:95], v[2:3], v[74:75] op_sel_hi:[1,0,1]\n"
  "v_cvt_pk_bf16_f32 v70, v72, v73\n"
  "v_cvt_pk_bf16_f32 v71, v74, v75\n"
  "s_nop 0\n"
  "v_permlane16_swap_b32 v68, v70\n"
  "v_permlane16_swap_b32 v69, v71\n"
  "global_store_dwordx4 v12, v[68:71], s[22:23]\n"
  "s_waitcnt lgkmcnt(0)\n"
  "v_pk_fma_f32 v[84:85], v[96:97], v[2:3], v[84:85] op_sel_hi:[1,0,1]\n"
  "v_pk_fma_f32 v[86:87], v[98:99], v[2:3], v[86:87] op_sel_hi:[1,0,1]\n"
  "v_cvt_pk_bf16_f32 v84, v84, v85\n"
  "v_cvt_pk_bf16_f32 v85, v86, v87\n"
  "global_store_dwordx2 v63, v[84:85], s[22:23] offset:128\n"
  "s_cmp_eq_u32 s37, 0\n"
  "s_cbranch_scc1 .Lbump\n"
  "s_endpgm\n"
  ".Lbump:\n"
  "s_mov_b32 s42, 0\n"
  "s_waitcnt vmcnt(2)\n"
  ".Lbp:\n"
  "v_cmp_ne_u64 vcc, s[62:63], v[24:25]\n"
  "v_cmp_ne_u64 s[58:59], s[62:63], v[26:27]\n"
  "s_nop 0\n"
  "s_or_b64 vcc, vcc, s[58:59]\n"
  "v_cmp_ne_u64 s[58:59], s[62:63], v[28:29]\n"
  "s_nop 0\n"
  "s_or_b64 vcc, vcc, s[58:59]\n"
  "v_cmp_ne_u64 s[58:59], s[62:63], v[30:31]\n"
  "s_nop 0\n"
  "s_or_b64 vcc, vcc, s[58:59]\n"
  "s_cmp_eq_u64 vcc, 0\n"
  "s_cbranch_scc1 .Lbd\n"
  "global_load_dwordx2 v[24:25], v4, s[20:21] sc1\n"
  "global_load_dwordx2 v[26:27], v4, s[20:21] offset:512 sc1\n"
  "global_load_dwordx2 v[28:29], v4, s[20:21] offset:1024 sc1\n"
  "global_load_dwordx2 v[30:31], v4, s[20:21] offset:1536 sc1\n"
  "s_waitcnt vmcnt(0)\n"
  "s_add_u32 s42, s42, 1\n"
  "s_cmp_lt_u32 s42, 8192\n"
  "s_cbranch_scc1 .Lbp\n"
  ".Lbd:\n"
  "v_mov_b32 v2, 5312512\n"
  "s_mov_b64 exec, 1\n"
  "global_store_dwordx2 v2, v[64:65], s[20:21] sc1\n"
  "s_mov_b64 exec, -1\n"
  "s_endpgm\n"
  ".Lpoll:\n"
  "s_sleep 1\n"
  "global_load_dwordx2 v[10:11], v9, s[20:21] sc1\n"
  "s_waitcnt vmcnt(0)\n"
  "v_cmp_ne_u64 vcc, s[62:63], v[10:11]\n"
  "s_add_u32 s42, s42, 1\n"
  "s_cmp_eq_u64 vcc, 0\n"
  "s_cbranch_scc1 .Lpolln\n"
  "s_cmp_lt_u32 s42, 8192\n"
  "s_cbranch_scc1 .Lpoll\n"
  "s_branch .Lpolln\n"
  ".Lsl0:\n"
  "s_add_u32 s41, s41, 1\n"
  "s_cmp_gt_u32 s41, 16384\n"
  "s_cbranch_scc1 .Lok0\n"
  "s_sleep 2\n"
  "s_mov_b32 s42, 1\n"
  "s_call_b64 s[64:65], .Lrl0\n"
  "s_waitcnt vmcnt(0)\n"
  "v_max3_u32 v2, v24, v25, v26\n"
  "v_max_u32 v2, v2, v27\n"
  "v_cmp_eq_u32 vcc, -1, v2\n"
  "s_nop 0\n"
  "s_cbranch_vccnz .Lsl0\n"
  "s_mov_b32 s42, 3\n"
  "s_call_b64 s[64:65], .Lrl1\n"
  "s_branch .Lok0\n"
  ".Lsl1:\n"
  "s_add_u32 s41, s41, 1\n"
  "s_cmp_gt_u32 s41, 16384\n"
  "s_cbranch_scc1 .Lok1\n"
  "s_sleep 2\n"
  "s_mov_b32 s42, 1\n"
  "s_call_b64 s[64:65], .Lrl1\n"
  "s_waitcnt vmcnt(0)\n"
  "v_max3_u32 v2, v28, v29, v30\n"
  "v_max_u32 v2, v2, v31\n"
  "v_cmp_eq_u32 vcc, -1, v2\n"
  "s_nop 0\n"
  "s_cbranch_vccnz .Lsl1\n"
  "s_mov_b32 s42, 3\n"
  "s_call_b64 s[64:65], .Lrl2\n"
  "s_branch .Lok1\n"
  ".Lsl2:\n"
  "s_add_u32 s41, s41, 1\n"
  "s_cmp_gt_u32 s41, 16384\n"
  "s_cbranch_scc1 .Lok2\n"
  "s_sleep 2\n"
  "s_mov_b32 s42, 1\n"
  "s_call_b64 s[64:65], .Lrl2\n"
  "s_waitcnt vmcnt(0)\n"
  "v_max3_u32 v2, v32, v33, v34\n"
  "v_max_u32 v2, v2, v35\n"
  "v_cmp_eq_u32 vcc, -1, v2\n"
  "s_nop 0\n"
  "s_cbranch_vccnz .Lsl2\n"
  "s_mov_b32 s42, 3\n"
  "s_call_b64 s[64:65], .Lrl3\n"
  "s_branch .Lok2\n"
  ".Lsl3:\n"
  "s_add_u32 s41, s41, 1\n"
  "s_cmp_gt_u32 s41, 16384\n"
  "s_cbranch_scc1 .Lok3\n"
  "s_sleep 2\n"
  "s_mov_b32 s42, 1\n"
  "s_call_b64 s[64:65], .Lrl3\n"
  "s_waitcnt vmcnt(0)\n"
  "v_max3_u32 v2, v36, v37, v38\n"
  "v_max_u32 v2, v2, v39\n"
  "v_cmp_eq_u32 vcc, -1, v2\n"
  "s_nop 0\n"
  "s_cbranch_vccnz .Lsl3\n"
  "s_mov_b32 s42, 3\n"
  "s_call_b64 s[64:65], .Lrl4\n"
  "s_branch .Lok3\n"
  ".Lsl4:\n"
  "s_add_u32 s41, s41, 1\n"
  "s_cmp_gt_u32 s41, 16384\n"
  "s_cbranch_scc1 .Lok4\n"
  "s_sleep 2\n"
  "s_mov_b32 s42, 1\n"
  "s_call_b64 s[64:65], .Lrl4\n"
  "s_waitcnt vmcnt(0)\n"
  "v_max3_u32 v2, v40, v41, v42\n"
  "v_max_u32 v2, v2, v43\n"
  "v_cmp_eq_u32 vcc, -1, v2\n"
  "s_nop 0\n"
  "s_cbranch_vccnz .Lsl4\n"
  "s_mov_b32 s42, 3\n"
  "s_call_b64 s[64:65], .Lrl5\n"
  "s_branch .Lok4\n"
  ".Lsl5:\n"
  "s_add_u32 s41, s41, 1\n"
  "s_cmp_gt_u32 s41, 16384\n"
  "s_cbranch_scc1 .Lok5\n"
  "s_sleep 2\n"
  "s_mov_b32 s42, 1\n"
  "s_call_b64 s[64:65], .Lrl5\n"
  "s_waitcnt vmcnt(0)\n"
  "v_max3_u32 v2, v44, v45, v46\n"
  "v_max_u32 v2, v2, v47\n"
  "v_cmp_eq_u32 vcc, -1, v2\n"
  "s_nop 0\n"
  "s_cbranch_vccnz .Lsl5\n"
  "s_mov_b32 s42, 3\n"
  "s_call_b64 s[64:65], .Lrl6\n"
  "s_branch .Lok5\n"
  ".Lsl6:\n"
  "s_add_u32 s41, s41, 1\n"
  "s_cmp_gt_u32 s41, 16384\n"
  "s_cbranch_scc1 .Lok6\n"
  "s_sleep 2\n"
  "s_mov_b32 s42, 1\n"
  "s_call_b64 s[64:65], .Lrl6\n"
  "s_waitcnt vmcnt(0)\n"
  "v_max3_u32 v2, v48, v49, v50\n"
  "v_max_u32 v2, v2, v51\n"
  "v_cmp_eq_u32 vcc, -1, v2\n"
  "s_nop 0\n"
  "s_cbranch_vccnz .Lsl6\n"
  "s_mov_b32 s42, 4\n"
  "s_call_b64 s[64:65], .Lrl7\n"
  "s_branch .Lok6\n"
  ".Lsl7:\n"
  "s_add_u32 s41, s41, 1\n"
  "s_cmp_gt_u32 s41, 16384\n"
  "s_cbranch_scc1 .Lok7\n"
  "s_sleep 2\n"
  "s_mov_b32 s42, 1\n"
  "s_call_b64 s[64:65], .Lrl7\n"
  "s_waitcnt vmcnt(0)\n"
  "v_max3_u32 v2, v52, v53, v54\n"
  "v_max_u32 v2, v2, v55\n"
  "v_cmp_eq_u32 vcc, -1, v2\n"
  "s_nop 0\n"
  "s_cbranch_vccnz .Lsl7\n"
  "s_mov_b32 s42, 4\n"
  "s_call_b64 s[64:65], .Lrl8\n"
  "s_branch .Lok7\n"
  ".Lsl8:\n"
  "s_add_u32 s41, s41, 1\n"
  "s_cmp_gt_u32 s41, 16384\n"
  "s_cbranch_scc1 .Lok8\n"
  "s_sleep 2\n"
  "s_mov_b32 s42, 1\n"
  "s_call_b64 s[64:65], .Lrl8\n"
  "s_waitcnt vmcnt(0)\n"
  "v_max3_u32 v2, v56, v57, v58\n"
  "v_max_u32 v2, v2, v59\n"
  "v_cmp_eq_u32 vcc, -1, v2\n"
  "s_nop 0\n"
  "s_cbranch_vccnz .Lsl8\n"
  "s_mov_b32 s42, 5\n"
  "s_call_b64 s[64:65], .Lrl9\n"
  "s_branch .Lok8\n"
  ".Lsl9:\n"
  "s_add_u32 s41, s41, 1\n"
  "s_cmp_gt_u32 s41, 16384\n"
  "s_cbranch_scc1 .Lok9\n"
  "s_sleep 2\n"
  "s_mov_b32 s42, 1\n"
  "s_call_b64 s[64:65], .Lrl9\n"
  "s_waitcnt vmcnt(0)\n"
  "v_max3_u32 v2, v8, v9, v10\n"
  "v_max_u32 v2, v2, v11\n"
  "v_cmp_eq_u32 vcc, -1, v2\n"
  "s_nop 0\n"
  "s_cbranch_vccnz .Lsl9\n"
  "s_mov_b32 s42, 5\n"
  "s_call_b64 s[64:65], .Lrl10\n"
  "s_branch .Lok9\n"
  ".Lsl10:\n"
  "s_add_u32 s41, s41, 1\n"
  "s_cmp_gt_u32 s41, 16384\n"
  "s_cbranch_scc1 .Lok10\n"
  "s_sleep 2\n"
  "s_mov_b32 s42, 1\n"
  "s_call_b64 s[64:65], .Lrl10\n"
  "s_waitcnt vmcnt(0)\n"
  "v_max3_u32 v2, v24, v25, v26\n"
  "v_max_u32 v2, v2, v27\n"
  "v_cmp_eq_u32 vcc, -1, v2\n"
  "s_nop 0\n"
  "s_cbranch_vccnz .Lsl10\n"
  "s_mov_b32 s42, 5\n"
  "s_call_b64 s[64:65], .Lrl11\n"
  "s_branch .Lok10\n"
  ".Lsl11:\n"
  "s_add_u32 s41, s41, 1\n"
  "s_cmp_gt_u32 s41, 16384\n"
  "s_cbranch_scc1 .Lok11\n"
  "s_sleep 2\n"
  "s_mov_b32 s42, 1\n"
  "s_call_b64 s[64:65], .Lrl11\n"
  "s_waitcnt vmcnt(0)\n"
  "v_max3_u32 v2, v28, v29, v30\n"
  "v_max_u32 v2, v2, v31\n"
  "v_cmp_eq_u32 vcc, -1, v2\n"
  "s_nop 0\n"
  "s_cbranch_vccnz .Lsl11\n"
  "s_mov_b32 s42, 5\n"
  "s_call_b64 s[64:65], .Lrl12\n"
  "s_branch .Lok11\n"
  ".Lsl12:\n"
  "s_add_u32 s41, s41, 1\n"
  "s_cmp_gt_u32 s41, 16384\n"
  "s_cbranch_scc1 .Lok12\n"
  "s_sleep 2\n"
  "s_mov_b32 s42, 1\n"
  "s_call_b64 s[64:65], .Lrl12\n"
  "s_waitcnt vmcnt(0)\n"
  "v_max3_u32 v2, v32, v33, v34\n"
  "v_max_u32 v2, v2, v35\n"
  "v_cmp_eq_u32 vcc, -1, v2\n"
  "s_nop 0\n"
  "s_cbranch_vccnz .Lsl12\n"
  "s_mov_b32 s42, 5\n"
  "s_call_b64 s[64:65], .Lrl13\n"
  "s_branch .Lok12\n"
  ".Lsl13:\n"
  "s_add_u32 s41, s41, 1\n"
  "s_cmp_gt_u32 s41, 16384\n"
  "s_cbranch_scc1 .Lok13\n"
  "s_sleep 2\n"
  "s_mov_b32 s42, 1\n"
  "s_call_b64 s[64:65], .Lrl13\n"
  "s_waitcnt vmcnt(0)\n"
  "v_max3_u32 v2, v36, v37, v38\n"
  "v_max_u32 v2, v2, v39\n"
  "v_cmp_eq_u32 vcc, -1, v2\n"
  "s_nop 0\n"
  "s_cbranch_vccnz .Lsl13\n"
  "s_mov_b32 s42, 5\n"
  "s_call_b64 s[64:65], .Lrl14\n"
  "s_branch .Lok13\n"
  ".Lsl14:\n"
  "s_add_u32 s41, s41, 1\n"
  "s_cmp_gt_u32 s41, 16384\n"
  "s_cbranch_scc1 .Lok14\n"
  "s_sleep 2\n"
  "s_mov_b32 s42, 1\n"
  "s_call_b64 s[64:65], .Lrl14\n"
  "s_waitcnt vmcnt(0)\n"
  "v_max3_u32 v2, v40, v41, v42\n"
  "v_max_u32 v2, v2, v43\n"
  "v_cmp_eq_u32 vcc, -1, v2\n"
  "s_nop 0\n"
  "s_cbranch_vccnz .Lsl14\n"
  "s_mov_b32 s42, 5\n"
  "s_call_b64 s[64:65], .Lrl15\n"
  "s_branch .Lok14\n"
  ".Lsl15:\n"
  "s_add_u32 s41, s41, 1\n"
  "s_cmp_gt_u32 s41, 16384\n"
  "s_cbranch_scc1 .Lok15\n"
  "s_sleep 2\n"
  "s_mov_b32 s42, 1\n"
  "s_call_b64 s[64:65], .Lrl15\n"
  "s_waitcnt vmcnt(0)\n"
  "v_max3_u32 v2, v44, v45, v46\n"
  "v_max_u32 v2, v2, v47\n"
  "v_cmp_eq_u32 vcc, -1, v2\n"
  "s_nop 0\n"
  "s_cbranch_vccnz .Lsl15\n"
  "s_mov_b32 s42, 4\n"
  "s_call_b64 s[64:65], .Lrl16\n"
  "s_branch .Lok15\n"
  ".Lsl16:\n"
  "s_add_u32 s41, s41, 1\n"
  "s_cmp_gt_u32 s41, 16384\n"
  "s_cbranch_scc1 .Lok16\n"
  "s_sleep 2\n"
  "s_mov_b32 s42, 1\n"
  "s_call_b64 s[64:65], .Lrl16\n"
  "s_waitcnt vmcnt(0)\n"
  "v_max3_u32 v2, v48, v49, v50\n"
  "v_max_u32 v2, v2, v51\n"
  "v_cmp_eq_u32 vcc, -1, v2\n"
  "s_nop 0\n"
  "s_cbranch_vccnz .Lsl16\n"
  "s_mov_b32 s42, 3\n"
  "s_call_b64 s[64:65], .Lrl17\n"
  "s_branch .Lok16\n"
  ".Lsl17:\n"
  "s_add_u32 s41, s41, 1\n"
  "s_cmp_gt_u32 s41, 16384\n"
  "s_cbranch_scc1 .Lok17\n"
  "s_sleep 2\n"
  "s_mov_b32 s42, 1\n"
  "s_call_b64 s[64:65], .Lrl17\n"
  "s_waitcnt vmcnt(0)\n"
  "v_max3_u32 v2, v52, v53, v54\n"
  "v_max_u32 v2, v2, v55\n"
  "v_cmp_eq_u32 vcc, -1, v2\n"
  "s_nop 0\n"
  "s_cbranch_vccnz .Lsl17\n"
  "s_mov_b32 s42, 2\n"
  "s_call_b64 s[64:65], .Lrl18\n"
  "s_branch .Lok17\n"
  ".Lsl18:\n"
  "s_add_u32 s41, s41, 1\n"
  "s_cmp_gt_u32 s41, 16384\n"
  "s_cbranch_scc1 .Lok18\n"
  "s_sleep 2\n"
  "s_mov_b32 s42, 1\n"
  "s_call_b64 s[64:65], .Lrl18\n"
  "s_waitcnt vmcnt(0)\n"
  "v_max3_u32 v2, v56, v57, v58\n"
  "v_max_u32 v2, v2, v59\n"
  "v_cmp_eq_u32 vcc, -1, v2\n"
  "s_nop 0\n"
  "s_cbranch_vccnz .Lsl18\n"
  "s_mov_b32 s42, 1\n"
  "s_call_b64 s[64:65], .Lrl19\n"
  "s_branch .Lok18\n"
  ".Lsl19:\n"
  "s_add_u32 s41, s41, 1\n"
  "s_cmp_gt_u32 s41, 16384\n"
  "s_cbranch_scc1 .Lok19\n"
  "s_sleep 2\n"
  "s_mov_b32 s42, 1\n"
  "s_call_b64 s[64:65], .Lrl19\n"
  "s_waitcnt vmcnt(0)\n"
  "v_max3_u32 v2, v8, v9, v10\n"
  "v_max_u32 v2, v2, v11\n"
  "v_cmp_eq_u32 vcc, -1, v2\n"
  "s_nop 0\n"
  "s_cbranch_vccnz .Lsl19\n"
  "s_branch .Lok19\n"
  ".Lrl0:\n"
  "s_add_u32 s32, s51, 0\n"
  "s_and_b32 s32, s32, 3\n"
  "s_lshl_b32 s32, s32, 10\n"
  "v_add_u32 v3, s32, v129\n"
  "global_load_dwordx2 v[24:25], v3, s[52:53] sc1\n"
  "global_load_dwordx2 v[26:27], v3, s[52:53] offset:512 sc1\n"
  "s_sub_u32 s42, s42, 1\n"
  "s_cmp_lg_u32 s42, 0\n"
  "s_cbranch_scc1 .Lrl1\n"
  "s_setpc_b64 s[64:65]\n"
  ".Lrl1:\n"
  "s_add_u32 s32, s51, 1\n"
  "s_and_b32 s32, s32, 3\n"
  "s_lshl_b32 s32, s32, 10\n"
  "v_add_u32 v3, s32, v129\n"
  "global_load_dwordx2 v[28:29], v3, s[52:53] sc1\n"
  "global_load_dwordx2 v[30:31], v3, s[52:53] offset:512 sc1\n"
  "s_sub_u32 s42, s42, 1\n"
  "s_cmp_lg_u32 s42, 0\n"
  "s_cbranch_scc1 .Lrl2\n"
  "s_setpc_b64 s[64:65]\n"
  ".Lrl2:\n"
  "s_add_u32 s32, s51, 2\n"
  "s_and_b32 s32, s32, 3\n"
  "s_lshl_b32 s32, s32, 10\n"
  "v_add_u32 v3, s32, v129\n"
  "global_load_dwordx2 v[32:33], v3, s[52:53] sc1\n"
  "global_load_dwordx2 v[34:35], v3, s[52:53] offset:512 sc1\n"
  "s_sub_u32 s42, s42, 1\n"
  "s_cmp_lg_u32 s42, 0\n"
  "s_cbranch_scc1 .Lrl3\n"
  "s_setpc_b64 s[64:65]\n"
  ".Lrl3:\n"
  "s_add_u32 s32, s51, 3\n"
  "s_and_b32 s32, s32, 3\n"
  "s_lshl_b32 s32, s32, 10\n"
  "v_add_u32 v3, s32, v129\n"
  "global_load_dwordx2 v[36:37], v3, s[52:53] sc1\n"
  "global_load_dwordx2 v[38:39], v3, s[52:53] offset:512 sc1\n"
  "s_sub_u32 s42, s42, 1\n"
  "s_cmp_lg_u32 s42, 0\n"
  "s_cbranch_scc1 .Lrl4\n"
  "s_setpc_b64 s[64:65]\n"
  ".Lrl4:\n"
  "s_add_u32 s32, s47, 0\n"
  "s_and_b32 s32, s32, 15\n"
  "s_lshl_b32 s32, s32, 10\n"
  "v_add_u32 v3, s32, v66\n"
  "global_load_dwordx4 v[40:43], v3, s[8:9] sc1\n"
  "s_sub_u32 s42, s42, 1\n"
  "s_cmp_lg_u32 s42, 0\n"
  "s_cbranch_scc1 .Lrl5\n"
  "s_setpc_b64 s[64:65]\n"
  ".Lrl5:\n"
  "s_add_u32 s32, s47, 0\n"
  "s_and_b32 s32, s32, 15\n"
  "s_lshl_b32 s32, s32, 10\n"
  "v_add_u32 v3, s32, v66\n"
  "global_load_dwordx4 v[44:47], v3, s[8:9] offset:1024 sc1\n"
  "s_sub_u32 s42, s42, 1\n"
  "s_cmp_lg_u32 s42, 0\n"
  "s_cbranch_scc1 .Lrl6\n"
  "s_setpc_b64 s[64:65]\n"
  ".Lrl6:\n"
  "s_add_u32 s32, s47, 2\n"
  "s_and_b32 s32, s32, 15\n"
  "s_lshl_b32 s32, s32, 10\n"
  "v_add_u32 v3, s32, v66\n"
  "global_load_dwordx4 v[48:51], v3, s[8:9] sc1\n"
  "s_sub_u32 s42, s42, 1\n"
  "s_cmp_lg_u32 s42, 0\n"
  "s_cbranch_scc1 .Lrl7\n"
  "s_setpc_b64 s[64:65]\n"
  ".Lrl7:\n"
  "s_add_u32 s32, s47, 2\n"
  "s_and_b32 s32, s32, 15\n"
  "s_lshl_b32 s32, s32, 10\n"
  "v_add_u32 v3, s32, v66\n"
  "global_load_dwordx4 v[52:55], v3, s[8:9] offset:1024 sc1\n"
  "s_sub_u32 s42, s42, 1\n"
  "s_cmp_lg_u32 s42, 0\n"
  "s_cbranch_scc1 .Lrl8\n"
  "s_setpc_b64 s[64:65]\n"
  ".Lrl8:\n"
  "s_add_u32 s32, s47, 4\n"
  "s_and_b32 s32, s32, 15\n"
  "s_lshl_b32 s32, s32, 10\n"
  "v_add_u32 v3, s32, v66\n"
  "global_load_dwordx4 v[56:59], v3, s[8:9] sc1\n"
  "s_sub_u32 s42, s42, 1\n"
  "s_cmp_lg_u32 s42, 0\n"
  "s_cbranch_scc1 .Lrl9\n"
  "s_setpc_b64 s[64:65]\n"
  ".Lrl9:\n"
  "s_add_u32 s32, s47, 4\n"
  "s_and_b32 s32, s32, 15\n"
  "s_lshl_b32 s32, s32, 10\n"
  "v_add_u32 v3, s32, v66\n"
  "global_load_dwordx4 v[8:11], v3, s[8:9] offset:1024 sc1\n"
  "s_sub_u32 s42, s42, 1\n"
  "s_cmp_lg_u32 s42, 0\n"
  "s_cbranch_scc1 .Lrl10\n"
  "s_setpc_b64 s[64:65]\n"
  ".Lrl10:\n"
  "s_add_u32 s32, s47, 6\n"
  "s_and_b32 s32, s32, 15\n"
  "s_lshl_b32 s32, s32, 10\n"
  "v_add_u32 v3, s32, v66\n"
  "global_load_dwordx4 v[24:27], v3, s[8:9] sc1\n"
  "s_sub_u32 s42, s42, 1\n"
  "s_cmp_lg_u32 s42, 0\n"
  "s_cbranch_scc1 .Lrl11\n"
  "s_setpc_b64 s[64:65]\n"
  ".Lrl11:\n"
  "s_add_u32 s32, s47, 6\n"
  "s_and_b32 s32, s32, 15\n"
  "s_lshl_b32 s32, s32, 10\n"
  "v_add_u32 v3, s32, v66\n"
  "global_load_dwordx4 v[28:31], v3, s[8:9] offset:1024 sc1\n"
  "s_sub_u32 s42, s42, 1\n"
  "s_cmp_lg_u32 s42, 0\n"
  "s_cbranch_scc1 .Lrl12\n"
  "s_setpc_b64 s[64:65]\n"
  ".Lrl12:\n"
  "s_add_u32 s32, s47, 8\n"
  "s_and_b32 s32, s32, 15\n"
  "s_lshl_b32 s32, s32, 10\n"
  "v_add_u32 v3, s32, v66\n"
  "global_load_dwordx4 v[32:35], v3, s[8:9] sc1\n"
  "s_sub_u32 s42, s42, 1\n"
  "s_cmp_lg_u32 s42, 0\n"
  "s_cbranch_scc1 .Lrl13\n"
  "s_setpc_b64 s[64:65]\n"
  ".Lrl13:\n"
  "s_add_u32 s32, s47, 8\n"
  "s_and_b32 s32, s32, 15\n"
  "s_lshl_b32 s32, s32, 10\n"
  "v_add_u32 v3, s32, v66\n"
  "global_load_dwordx4 v[36:39], v3, s[8:9] offset:1024 sc1\n"
  "s_sub_u32 s42, s42, 1\n"
  "s_cmp_lg_u32 s42, 0\n"
  "s_cbranch_scc1 .Lrl14\n"
  "s_setpc_b64 s[64:65]\n"
  ".Lrl14:\n"
  "s_add_u32 s32, s47, 10\n"
  "s_and_b32 s32, s32, 15\n"
  "s_lshl_b32 s32, s32, 10\n"
  "v_add_u32 v3, s32, v66\n"
  "global_load_dwordx4 v[40:43], v3, s[8:9] sc1\n"
  "s_sub_u32 s42, s42, 1\n"
  "s_cmp_lg_u32 s42, 0\n"
  "s_cbranch_scc1 .Lrl15\n"
  "s_setpc_b64 s[64:65]\n"
  ".Lrl15:\n"
  "s_add_u32 s32, s47, 10\n"
  "s_and_b32 s32, s32, 15\n"
  "s_lshl_b32 s32, s32, 10\n"
  "v_add_u32 v3, s32, v66\n"
  "global_load_dwordx4 v[44:47], v3, s[8:9] offset:1024 sc1\n"
  "s_sub_u32 s42, s42, 1\n"
  "s_cmp_lg_u32 s42, 0\n"
  "s_cbranch_scc1 .Lrl16\n"
  "s_setpc_b64 s[64:65]\n"
  ".Lrl16:\n"
  "s_add_u32 s32, s47, 12\n"
  "s_and_b32 s32, s32, 15\n"
  "s_lshl_b32 s32, s32, 10\n"
  "v_add_u32 v3, s32, v66\n"
  "global_load_dwordx4 v[48:51], v3, s[8:9] sc1\n"
  "s_sub_u32 s42, s42, 1\n"
  "s_cmp_lg_u32 s42, 0\n"
  "s_cbranch_scc1 .Lrl17\n"
  "s_setpc_b64 s[64:65]\n"
  ".Lrl17:\n"
  "s_add_u32 s32, s47, 12\n"
  "s_and_b32 s32, s32, 15\n"
  "s_lshl_b32 s32, s32, 10\n"
  "v_add_u32 v3, s32, v66\n"
  "global_load_dwordx4 v[52:55], v3, s[8:9] offset:1024 sc1\n"
  "s_sub_u32 s42, s42, 1\n"
  "s_cmp_lg_u32 s42, 0\n"
  "s_cbranch_scc1 .Lrl18\n"
  "s_setpc_b64 s[64:65]\n"
  ".Lrl18:\n"
  "s_add_u32 s32, s47, 14\n"
  "s_and_b32 s32, s32, 15\n"
  "s_lshl_b32 s32, s32, 10\n"
  "v_add_u32 v3, s32, v66\n"
  "global_load_dwordx4 v[56:59], v3, s[8:9] sc1\n"
  "s_sub_u32 s42, s42, 1\n"
  "s_cmp_lg_u32 s42, 0\n"
  "s_cbranch_scc1 .Lrl19\n"
  "s_setpc_b64 s[64:65]\n"
  ".Lrl19:\n"
  "s_add_u32 s32, s47, 14\n"
  "s_and_b32 s32, s32, 15\n"
  "s_lshl_b32 s32, s32, 10\n"
  "v_add_u32 v3, s32, v66\n"
  "global_load_dwordx4 v[8:11], v3, s[8:9] offset:1024 sc1\n"
  "global_load_dword v134, v128, s[20:21] sc1\n"
  "s_setpc_b64 s[64:65]\n"
  ".Lslg:\n"
  "s_add_u32 s41, s41, 1\n"
  "s_cmp_gt_u32 s41, 16384\n"
  "s_cbranch_scc1 .Lokg\n"
  "global_load_dword v134, v128, s[20:21] sc1\n"
  "s_waitcnt vmcnt(0)\n"
  "v_cmp_eq_u32 vcc, -1, v134\n"
  "s_nop 0\n"
  "s_cbranch_vccnz .Lslg\n"
  "s_branch .Lokg\n"
  ".Lend:\n"
  :: "s"(__builtin_amdgcn_kernarg_segment_ptr()), "s"(blockIdx.x), "v"(threadIdx.x)
  : "v1", "v2", "v3", "v4", "v5", "v6", "v7", "v8", "v9", "v10", "v11", "v12", "v13", "v14", "v15", "v16", "v17", "v18", "v19", "v20", "v21", "v22", "v23", "v24", "v25", "v26", "v27", "v28", "v29", "v30", "v31", "v32", "v33", "v34", "v35", "v36", "v37", "v38", "v39", "v40", "v41", "v42", "v43", "v44", "v45", "v46", "v47", "v48", "v49", "v50", "v51", "v52", "v53", "v54", "v55", "v56", "v57", "v58", "v59", "v60", "v61", "v62", "v63", "v64", "v65", "v66", "v67", "v68", "v69", "v70", "v71", "v72", "v73", "v74", "v75", "v76", "v77", "v78", "v79", "v80", "v81", "v82", "v83", "v84", "v85", "v86", "v87", "v88", "v89", "v90", "v91", "v92", "v93", "v94", "v95", "v96", "v97", "v98", "v99", "v100", "v101", "v102", "v103", "v104", "v105", "v106", "v107", "v108", "v109", "v110", "v111", "v112", "v113", "v114", "v115", "v116", "v117", "v118", "v119", "v120", "v121", "v122", "v123", "v124", "v125", "v126", "v127", "v128", "v129", "v130", "v131", "v132", "v133", "v134", "v135", "v136", "v137", "v138", "v139", "v140", "v141", "v142", "v143", "v144", "v145", "v146", "v147", "v148", "v149", "v150", "v151", "v152", "v153", "v154", "v155", "v156", "v157", "v158", "v159", "v160", "v161", "v162", "v163", "v164", "v165", "v166", "v167", "v168", "v169", "v170", "v171", "v172", "v173", "v174", "v175", "v176", "v177", "v178", "v179", "v180", "v181", "v182", "v183", "v184", "v185", "v186", "v187", "v188", "v189", "v190", "v191", "v192", "v193", "v194", "v195", "v196", "v197", "v198", "v199", "v200", "v201", "v202", "v203", "v204", "v205", "v206", "v207", "v208", "v209", "v210", "v211", "v212", "v213", "v214", "v215", "v216", "v217", "v218", "v219", "v220", "v221", "v222", "v223", "v224", "v225", "v226", "v227", "v228", "v229", "v230", "v231", "v232", "v233", "v234", "v235", "v236", "v237", "v238", "v239", "v240", "v241", "v242", "v243", "v244", "v245", "v246", "v247", "v248", "v249", "v250", "v251", "v252", "v253", "v254", "v255", "s4", "s5", "s6", "s7", "s8", "s9", "s10", "s11", "s12", "s13", "s14", "s15", "s16", "s17", "s18", "s19", "s20", "s21", "s22", "s23", "s24", "s25", "s26", "s27", "s28", "s29", "s30", "s31", "s32", "s33", "s34", "s35", "s36", "s37", "s38", "s39", "s40", "s41", "s42", "s43", "s44", "s45", "s46", "s47", "s48", "s49", "s50", "s51", "s52", "s53", "s54", "s55", "s56", "s57", "s58", "s59", "s60", "s61", "s62", "s63", "s64", "s65", "s66", "s67", "s68", "s69", "s70", "s71", "s72", "s73", "vcc", "scc", "memory");
}

extern "C" void kernel_launch(void* const* d_in, const int* in_sizes, int n_in, void* d_out, int out_size, void* d_ws, size_t ws_size,
                              hipStream_t stream) {
  (void)hipFuncSetAttribute((const void*)k_fused, hipFuncAttributeMaxDynamicSharedMemorySize, 163840);
  k_fused<<<256, 512, 163840, stream>>>(d_in[0], d_in[1], d_in[4], d_in[6], d_in[2], d_in[5], d_ws, d_out);
}
```
